```python
import torch

def _softmax(x, axis=-1):
    return torch.softmax(x, dim=axis)
def _gelu(x):
    return torch.nn.functional.gelu(x, approximate="tanh")
def _silu(x):
    return torch.nn.functional.silu(x)
def _sigmoid(x):
    return torch.sigmoid(x)
def _layernorm(x, axis=-1, eps=1e-5):
    m = x.mean(dim=axis, keepdim=True)
    v = x.var(dim=axis, keepdim=True, unbiased=False)
    return (x - m) / torch.sqrt(v + eps)
def _scatter_add(tgt, idx, src, axis=0):
    return tgt.clone().index_add_(axis, idx, src)
def _segment_sum(src, seg, n):
    out = torch.zeros((n,) + src.shape[1:], dtype=src.dtype)
    return out.index_add_(0, seg, src)
def _pad(x, pw):
    flat = tuple(v for pair in reversed(pw) for v in pair)
    return torch.nn.functional.pad(x, flat)

kernel_name = 'gated_mix__softmax_full_mha__swiglu_ffn__prompt'

def setup_inputs():

    _w = (lambda a: (lambda t: t.to(torch.bfloat16) if t.is_floating_point() else t)(torch.as_tensor(a)))
    def _A_setup():
        B, S, D = 1, 2048, 1024
        return {
            "x": _w((lambda *s: torch.randn(*s, dtype=torch.bfloat16) * 0.05)(B, S, D)),
            "wqkv": _w((lambda *s: torch.randn(*s, dtype=torch.bfloat16) * 0.8)(D, 3 * D)),
            "wo": _w((lambda *s: torch.randn(*s, dtype=torch.bfloat16) * 0.05)(D, D)),
        }
    _A_in = _A_setup()

    def _B_setup():
        B, S, D, F = 1, 2048, 1024, 3584
        return {
            "x": _w((lambda *s: torch.randn(*s, dtype=torch.bfloat16) * 0.05)(B, S, D)),
            "w_gate": _w((lambda *s: torch.randn(*s, dtype=torch.bfloat16) * 0.1)(D, F)),
            "w_up": _w((lambda *s: torch.randn(*s, dtype=torch.bfloat16) * 0.1)(D, F)),
            "w_down": _w((lambda *s: torch.randn(*s, dtype=torch.bfloat16) * 0.1)(F, D)),
        }
    _B_in = _B_setup()

    return {
        "A_x": _A_in["x"],
        "A_wqkv": _A_in["wqkv"],
        "A_wo": _A_in["wo"],
        "B_x": _B_in["x"],
        "B_w_gate": _B_in["w_gate"],
        "B_w_up": _B_in["w_up"],
        "B_w_down": _B_in["w_down"],
        "gate_w": _w((lambda *s: torch.randn(*s, dtype=torch.bfloat16) * 0.05)(1024) * 16.0),
    }

def reference(A_x, A_wqkv, A_wo, B_x, B_w_gate, B_w_up, B_w_down, gate_w):
    def _A_ref(x, wqkv, wo):
        B, S, D = x.shape
        H = 32; Dh = D // H
        qkv = (lambda x, *d: x.permute(*d))((x @ wqkv).reshape(B, S, 3, H, Dh), 2, 0, 3, 1, 4)
        q, k, v = qkv[0], qkv[1], qkv[2]
        scale = Dh ** -0.5
        scores = torch.einsum("bhqd,bhkd->bhqk", q, k) * scale
        p = _softmax(scores, axis=-1)
        o = torch.einsum("bhqk,bhkd->bhqd", p, v)
        return (lambda x, *d: x.permute(*d))(o, 0, 2, 1, 3).reshape(B, S, D) @ wo

    def _B_ref(x, w_gate, w_up, w_down):
        g = _silu(x @ w_gate)
        u = x @ w_up
        return (g * u) @ w_down

    x = A_x
    g = _sigmoid((x * gate_w).sum(-1, keepdims=True))
    a = _A_ref(x, A_wqkv, A_wo)
    b = _B_ref(x, B_w_gate, B_w_up, B_w_down)
    return g * a + (1 - g) * b
```

```cpp
#pragma clang diagnostic ignored "-Winline-asm"
#include <hip/hip_runtime.h>
#include <stdint.h>

typedef __bf16 bf16x8 __attribute__((ext_vector_type(8)));
typedef __bf16 bf16x2 __attribute__((ext_vector_type(2)));
typedef short s16x4 __attribute__((ext_vector_type(4)));
typedef float f32x16 __attribute__((ext_vector_type(16)));
typedef float f32x4 __attribute__((ext_vector_type(4)));
typedef float f32x2 __attribute__((ext_vector_type(2)));
typedef unsigned u32x4 __attribute__((ext_vector_type(4)));
typedef unsigned u32x2 __attribute__((ext_vector_type(2)));
#define LAS __attribute__((address_space(3)))
#define DI __device__ __forceinline__
#define SBAR() __builtin_amdgcn_sched_barrier(0)
#define MFMA32(a, b, c) __builtin_amdgcn_mfma_f32_32x32x16_bf16((a), (b), (c), 0, 0, 0)
#define MFMA16(a, b, c) __builtin_amdgcn_mfma_f32_16x16x32_bf16((a), (b), (c), 0, 0, 0)

constexpr int SEQ = 2048, DM = 1024, FF = 3584, NH = 32;
constexpr int NT32 = SEQ / 32;
constexpr int KS_O = DM / 16;
constexpr int KS_H = FF / 16;
constexpr int KS_A = KS_O + KS_H;
constexpr size_t WS_Q = 0;
constexpr size_t WS_K = WS_Q + (size_t)NH * NT32 * 2048;
constexpr size_t WS_V = WS_K + (size_t)NH * NT32 * 2048;
constexpr size_t WS_A = WS_V + (size_t)NH * NT32 * 2048;
constexpr size_t WS_G = WS_A + (size_t)NT32 * KS_A * 1024;
constexpr size_t WS_END = WS_G + SEQ * 4;

DI unsigned cvtpk(float lo, float hi) { f32x2 v = {lo, hi}; bf16x2 b = __builtin_convertvector(v, bf16x2); return __builtin_bit_cast(unsigned, b); }
DI u32x4 pack8(const f32x16& a, int o, float s) {
  u32x4 w; w[0] = cvtpk(a[o + 0] * s, a[o + 1] * s); w[1] = cvtpk(a[o + 2] * s, a[o + 3] * s); w[2] = cvtpk(a[o + 4] * s, a[o + 5] * s); w[3] = cvtpk(a[o + 6] * s, a[o + 7] * s); return w; }
DI bf16x8 tr_frag(LAS const char* p0, LAS const char* p1) {
  s16x4 lo = __builtin_amdgcn_ds_read_tr16_b64_v4i16((LAS s16x4*)p0);
  s16x4 hi = __builtin_amdgcn_ds_read_tr16_b64_v4i16((LAS s16x4*)p1);
  return __builtin_bit_cast(bf16x8, __builtin_shufflevector(lo, hi, 0, 1, 2, 3, 4, 5, 6, 7)); }
DI void glds16(const char* sbase, unsigned voff, unsigned ldst) {
  asm volatile("s_mov_b32 m0, %0\n\ts_nop 0\n\tglobal_load_lds_dwordx4 %1, %2" ::"s"(ldst), "v"(voff), "s"(sbase) : "memory", "m0"); }
#define STAMP_DECL(kid)
#define STAMP_AT(i)
#define STAMP_END()
#define WAIT_VM(n) asm volatile("s_waitcnt vmcnt(" #n ")" ::: "memory")
#define WAIT_ALL_BAR() asm volatile("s_waitcnt vmcnt(0) lgkmcnt(0)\n\ts_barrier" ::: "memory")

namespace k1 {
constexpr int X_OFF = 0, WQ_OFF = 32768, WG_OFF = 45056, WU_OFF = 59520, STAGE = 73984, LDS_BYTES = 2 * STAGE;
constexpr float QSCALE = 0.17677669529663687f * 1.4426950408889634f;

template <int WC> DI void body(LAS char* lds, const char* x, const char* wqkv, const char* wg, const char* wu, char* ws, int j, int r, int wid, int lane) {
  STAMP_DECL(0); STAMP_AT(0);
  const int wr = wid & 3, hi = lane >> 5, l31 = lane & 31;
  const unsigned ldsb = (unsigned)(uintptr_t)lds;
  const char* xsrc = x + (size_t)(r * 256 + wid * 32) * 2048;
  unsigned xoff[2];
#pragma unroll
  for (int e = 0; e < 2; ++e) xoff[e] = (unsigned)((lane >> 3) * 2048 + (((lane & 7) ^ ((4 * e + (lane >> 4)) & 7)) << 4));
  const char* wsrc[5]; unsigned woff[5], wdst[5], wstep[5];
#pragma unroll
  for (int m = 0; m < 5; ++m) {
    const int p = wid + 8 * m;
    if (p < 12) { const int c = 64 * p + lane, row = c / 12, pos = c % 12; wsrc[m] = wqkv; woff[m] = (unsigned)(row * 6144 + ((pos >> 2) * 1024 + j * 32 + (pos & 3) * 8) * 2); wdst[m] = WQ_OFF + p * 1024; wstep[m] = 64 * 6144; }
    else { const int pp = p < 26 ? p - 12 : p - 26; const int c = 64 * pp + lane, row = c / 14, pos = c % 14; wsrc[m] = p < 26 ? wg : wu; woff[m] = (unsigned)(row * 7168 + (j * 112 + pos * 8) * 2);
           wdst[m] = (p < 26 ? WG_OFF : WU_OFF) + pp * 1024; wstep[m] = 64 * 7168; }
  }
#define K1_DMA_X(q, t, st) glds16(xsrc + (size_t)(t) * 128 + (q) * 8 * 2048, xoff[(q) & 1], ldsb + (st) + X_OFF + (wid * 4 + (q)) * 1024)
#define K1_DMA_W(m, t, st) glds16(wsrc[m] + (size_t)(t) * wstep[m], woff[m], ldsb + (st) + wdst[m])
  unsigned xa[4];
#pragma unroll
  for (int ks = 0; ks < 4; ++ks) xa[ks] = (unsigned)(X_OFF + (wr * 64 + l31) * 128 + (((2 * ks + hi) ^ ((l31 >> 1) & 7)) << 4));
  const int q4 = (lane & 15) >> 2, p4 = lane & 3, h16 = (lane >> 4) & 1;
  const unsigned wqa = (unsigned)(WQ_OFF + (8 * hi + q4) * 192 + 32 * h16 + 8 * p4);
  const unsigned wga = (unsigned)((h16 ? WU_OFF : WG_OFF) + (8 * hi + q4) * 224 + 8 * p4);
  f32x16 acc[2][5];
#pragma unroll
  for (int u = 0; u < 2; ++u)
#pragma unroll
    for (int b = 0; b < 5; ++b)
#pragma unroll
      for (int i = 0; i < 16; ++i) acc[u][b][i] = 0.f;
  bf16x8 xf[2][2], wf[2][5];
  auto ldx = [&](int set, int u, unsigned st, int ks) { xf[set][u] = *(LAS const bf16x8*)(lds + st + xa[ks] + u * 4096); };
  auto ldw = [&](int set, int b, unsigned st, int ks) {
    if (WC == 0 && b < 3) { LAS const char* p = lds + st + wqa + ks * 16 * 192 + b * 64; wf[set][b] = tr_frag(p, p + 4 * 192); }
    else { const int gb = WC == 0 ? b - 3 : b + 2; LAS const char* p = lds + st + wga + ks * 16 * 224 + gb * 32; wf[set][b] = tr_frag(p, p + 4 * 224); } };
  auto mma = [&](int set, int u, int b) {
    if (WC == 0 && b == 2) acc[u][b] = MFMA32(xf[set][u], wf[set][b], acc[u][b]); else acc[u][b] = MFMA32(wf[set][b], xf[set][u], acc[u][b]); };
#define K1_STEP(cs, nst, nks, DMA0, DMA1, DMA2, DMA3, DMA4) do { \
    ldx((cs) ^ 1, 0, nst, nks); ldx((cs) ^ 1, 1, nst, nks); mma(cs, 0, 0); DMA0; SBAR(); \
    ldw((cs) ^ 1, 0, nst, nks); mma(cs, 1, 0); SBAR(); mma(cs, 0, 1); DMA1; SBAR(); \
    ldw((cs) ^ 1, 1, nst, nks); mma(cs, 1, 1); SBAR(); mma(cs, 0, 2); DMA2; SBAR(); \
    ldw((cs) ^ 1, 2, nst, nks); mma(cs, 1, 2); SBAR(); mma(cs, 0, 3); DMA3; SBAR(); \
    ldw((cs) ^ 1, 3, nst, nks); mma(cs, 1, 3); SBAR(); ldw((cs) ^ 1, 4, nst, nks); mma(cs, 0, 4); DMA4; SBAR(); \
    mma(cs, 1, 4); SBAR(); } while (0)
#define NOP (void)0
#pragma unroll
  for (int q = 0; q < 4; ++q) K1_DMA_X(q, 0, 0);
#pragma unroll
  for (int m = 0; m < 5; ++m) K1_DMA_W(m, 0, 0);
#pragma unroll
  for (int q = 0; q < 4; ++q) K1_DMA_X(q, 1, STAGE);
#pragma unroll
  for (int m = 0; m < 5; ++m) K1_DMA_W(m, 1, STAGE);
  asm volatile("s_waitcnt vmcnt(9)\n\ts_barrier" ::: "memory");
  STAMP_AT(1);
  ldx(0, 0, 0, 0); ldx(0, 1, 0, 0);
#pragma unroll
  for (int b = 0; b < 5; ++b) ldw(0, b, 0, 0);
  unsigned cur = 0;
  for (int t = 0; t < 16; ++t) {
    const unsigned nxt = STAGE - cur;
    const int t2 = t + 2 < 16 ? t + 2 : 15;
    if (t > 0) K1_STEP(0, cur, 1, K1_DMA_W(1, t + 1 < 16 ? t + 1 : 15, nxt), K1_DMA_W(2, t + 1 < 16 ? t + 1 : 15, nxt), K1_DMA_W(3, t + 1 < 16 ? t + 1 : 15, nxt), K1_DMA_W(4, t + 1 < 16 ? t + 1 : 15, nxt), NOP);
    else K1_STEP(0, cur, 1, NOP, NOP, NOP, NOP, NOP);
    K1_STEP(1, cur, 2, NOP, NOP, NOP, NOP, NOP);
    K1_STEP(0, cur, 3, NOP, NOP, NOP, NOP, NOP);
    WAIT_ALL_BAR();
    K1_STEP(1, nxt, 0, K1_DMA_X(0, t2, cur), K1_DMA_X(1, t2, cur), K1_DMA_X(2, t2, cur), K1_DMA_X(3, t2, cur), K1_DMA_W(0, t2, cur));
    cur = nxt;
  }
  WAIT_VM(0);
  STAMP_AT(2);
#pragma unroll
  for (int u = 0; u < 2; ++u) {
    const int T = r * 8 + wr * 2 + u;
    if (WC == 0) {
      char* qp = ws + WS_Q + ((size_t)(j * NT32 + T) * 2048) + lane * 16;
      *(u32x4*)(qp) = pack8(acc[u][0], 0, QSCALE); *(u32x4*)(qp + 1024) = pack8(acc[u][0], 8, QSCALE);
      char* kp = ws + WS_K + ((size_t)(j * NT32 + T) * 2048) + lane * 16;
      *(u32x4*)(kp) = pack8(acc[u][1], 0, 1.f); *(u32x4*)(kp + 1024) = pack8(acc[u][1], 8, 1.f);
      char* vp = ws + WS_V + ((size_t)(j * NT32 + T) * 2048) + lane * 16;
      *(u32x4*)(vp) = pack8(acc[u][2], 0, 1.f); *(u32x4*)(vp + 1024) = pack8(acc[u][2], 8, 1.f);
    }
#pragma unroll
    for (int b = (WC == 0 ? 3 : 0); b < 5; ++b) {
      const int gb = WC == 0 ? b - 3 : b + 2;
      float h[8];
#pragma unroll
      for (int i = 0; i < 8; ++i) { const float g = acc[u][b][i], up = acc[u][b][8 + i];
        h[i] = g * __builtin_amdgcn_rcpf(1.f + __builtin_amdgcn_exp2f(-1.4426950408889634f * g)) * up; }
      u32x4 w; w[0] = cvtpk(h[0], h[1]); w[1] = cvtpk(h[2], h[3]); w[2] = cvtpk(h[4], h[5]); w[3] = cvtpk(h[6], h[7]);
      *(u32x4*)(ws + WS_A + ((size_t)T * KS_A + KS_O + j * 7 + gb) * 1024 + lane * 16) = w;
    }
  }
  STAMP_AT(3);
  STAMP_AT(4); STAMP_END();
}
__global__ __launch_bounds__(512, 2) void kernel(const char* x, const char* wqkv, const char* wg, const char* wu, const char* gatew, char* ws) {
  extern __shared__ __attribute__((aligned(16))) char smem[];
  LAS char* lds = (LAS char*)smem;
  const int tid = threadIdx.x, lane = tid & 63, wid = __builtin_amdgcn_readfirstlane(tid >> 6);
  const int w = blockIdx.x, xcd = w & 7, i = w >> 3, j = xcd * 4 + (i & 3), r = i >> 2;
  {
    const int tok = w * 8 + wid;
    const u32x4* xp = (const u32x4*)(x + (size_t)tok * 2048) + lane * 2; const u32x4* gp = (const u32x4*)gatew + lane * 2;
    const u32x4 a0 = xp[0], a1 = xp[1], b0 = gp[0], b1 = gp[1]; float s = 0.f;
#pragma unroll
    for (int e = 0; e < 4; ++e) { const unsigned ua0 = a0[e], ub0 = b0[e], ua1 = a1[e], ub1 = b1[e];
      s = __builtin_amdgcn_fdot2_f32_bf16(__builtin_bit_cast(bf16x2, ua0), __builtin_bit_cast(bf16x2, ub0), s, false);
      s = __builtin_amdgcn_fdot2_f32_bf16(__builtin_bit_cast(bf16x2, ua1), __builtin_bit_cast(bf16x2, ub1), s, false); }
#pragma unroll
    for (int o = 32; o >= 1; o >>= 1) s += __shfl_xor(s, o);
    if (lane == 0) ((float*)(ws + WS_G))[tok] = __builtin_amdgcn_rcpf(1.f + __builtin_amdgcn_exp2f(-1.4426950408889634f * s));
  }
  if (wid < 4) body<0>(lds, x, wqkv, wg, wu, ws, j, r, wid, lane); else body<1>(lds, x, wqkv, wg, wu, ws, j, r, wid, lane);
}
}

namespace k2 {
constexpr int TILE_KEYS = 128, NTILE = SEQ / TILE_KEYS, STAGE = 16384, NSTAGE = 3, LDS_BYTES = NSTAGE * STAGE;
DI float halfmax(float m) { auto rr = __builtin_amdgcn_permlane32_swap(__float_as_uint(m), __float_as_uint(m), false, false); return fmaxf(__uint_as_float(rr[0]), __uint_as_float(rr[1])); }
__global__ __launch_bounds__(512, 2) void kernel(char* ws) {
  extern __shared__ __attribute__((aligned(16))) char smem[];
  LAS char* lds = (LAS char*)smem;
  const int tid = threadIdx.x, lane = tid & 63, wid = __builtin_amdgcn_readfirstlane(tid >> 6);
  const int w = blockIdx.x, xcd = w & 7, i = w >> 3, hd = xcd * 4 + (i >> 3), r = i & 7;
  STAMP_DECL(1); STAMP_AT(0);
  const int T = r * 8 + wid;
  const char* qp = ws + WS_Q + ((size_t)(hd * NT32 + T) * 2048) + lane * 16;
  const char* kbase = ws + WS_K + (size_t)hd * NT32 * 2048; const char* vbase = ws + WS_V + (size_t)hd * NT32 * 2048;
  const unsigned ldsb = (unsigned)(uintptr_t)lds, l16 = lane * 16;
#define K2_DMA(t, st) do { glds16(kbase + (size_t)(t) * 8192 + wid * 1024, l16, ldsb + (st) + wid * 1024); glds16(vbase + (size_t)(t) * 8192 + wid * 1024, l16, ldsb + (st) + 8192 + wid * 1024); } while (0)
  K2_DMA(0, 0); K2_DMA(1, STAGE);
  const bf16x8 q0 = *(const bf16x8*)qp, q1 = *(const bf16x8*)(qp + 1024);
  const float gq = ((const float*)(ws + WS_G))[T * 32 + (lane & 31)];
  asm volatile("s_waitcnt vmcnt(2)\n\ts_barrier" ::: "memory");
  STAMP_AT(1);
  f32x16 negm;
  {
    float m = -INFINITY;
#pragma unroll
    for (int b = 0; b < 4; ++b) {
      const bf16x8 k0 = *(LAS const bf16x8*)(lds + b * 2048 + lane * 16), k1 = *(LAS const bf16x8*)(lds + b * 2048 + 1024 + lane * 16);
      f32x16 s = {}; s = MFMA32(k0, q0, s); s = MFMA32(k1, q1, s);
#pragma unroll
      for (int e = 0; e < 16; ++e) m = fmaxf(m, s[e]);
    }
    m = halfmax(m);
#pragma unroll
    for (int e = 0; e < 16; ++e) negm[e] = -m;
  }
  bf16x8 ones; { const bool on = ((lane & 1) == ((lane >> 4) & 1)); const __bf16 o = on ? (__bf16)1.0f : (__bf16)0.0f;
#pragma unroll
    for (int e = 0; e < 8; ++e) ones[e] = o; }
  f32x16 o = {}; f32x4 ls = {};
  unsigned st = 0;
  STAMP_AT(2);
  for (int t = 0; t < NTILE; ++t) {
    const unsigned st2 = st >= STAGE ? st - STAGE : st + 2 * STAGE;
    K2_DMA(t + 2 < NTILE ? t + 2 : NTILE - 1, st2);
#pragma unroll
    for (int b = 0; b < 4; ++b) {
      const bf16x8 k0 = *(LAS const bf16x8*)(lds + st + b * 2048 + lane * 16), k1 = *(LAS const bf16x8*)(lds + st + b * 2048 + 1024 + lane * 16);
      const bf16x8 v0 = *(LAS const bf16x8*)(lds + st + 8192 + b * 2048 + lane * 16), v1 = *(LAS const bf16x8*)(lds + st + 8192 + b * 2048 + 1024 + lane * 16);
      f32x16 s = MFMA32(k0, q0, negm); s = MFMA32(k1, q1, s);
#pragma unroll
      for (int e = 0; e < 16; ++e) s[e] = __builtin_amdgcn_exp2f(s[e]);
      const bf16x8 p0 = __builtin_bit_cast(bf16x8, pack8(s, 0, 1.f)), p1 = __builtin_bit_cast(bf16x8, pack8(s, 8, 1.f));
      o = MFMA32(v0, p0, o); o = MFMA32(v1, p1, o);
      ls = MFMA16(ones, p0, ls); ls = MFMA16(ones, p1, ls);
    }
    asm volatile("s_waitcnt vmcnt(2) lgkmcnt(0)\n\ts_barrier" ::: "memory");
    st = st == 2 * STAGE ? 0 : st + STAGE;
  }
  WAIT_VM(0);
  STAMP_AT(3);
  float l = (lane & 16) ? ls[1] : ls[0];
  float amax = 0.f;
#pragma unroll
  for (int e = 0; e < 16; ++e) amax = fmaxf(amax, fabsf(o[e]));
  const bool bad = !(l < 1.0995116e12f) || !(amax < INFINITY);
  if (__builtin_expect(__any(bad), 0)) {
    float m = -INFINITY; l = 0.f;
#pragma unroll
    for (int e = 0; e < 16; ++e) o[e] = 0.f;
    for (int b = 0; b < NT32; ++b) {
      const bf16x8 k0 = *(const bf16x8*)(kbase + b * 2048 + lane * 16), k1 = *(const bf16x8*)(kbase + b * 2048 + 1024 + lane * 16);
      const bf16x8 v0 = *(const bf16x8*)(vbase + b * 2048 + lane * 16), v1 = *(const bf16x8*)(vbase + b * 2048 + 1024 + lane * 16);
      f32x16 s = {}; s = MFMA32(k0, q0, s); s = MFMA32(k1, q1, s);
      float mb = s[0];
#pragma unroll
      for (int e = 1; e < 16; ++e) mb = fmaxf(mb, s[e]);
      const float mn = fmaxf(m, halfmax(mb)); const float al = __builtin_amdgcn_exp2f(m - mn); m = mn;
#pragma unroll
      for (int e = 0; e < 16; ++e) { s[e] = __builtin_amdgcn_exp2f(s[e] - mn); o[e] *= al; }
      const u32x4 w0 = pack8(s, 0, 1.f), w1 = pack8(s, 8, 1.f);
      float rs = 0.f;
#pragma unroll
      for (int e = 0; e < 4; ++e) rs += __uint_as_float(w0[e] << 16) + __uint_as_float(w0[e] & 0xffff0000u) + __uint_as_float(w1[e] << 16) + __uint_as_float(w1[e] & 0xffff0000u);
      { auto rr = __builtin_amdgcn_permlane32_swap(__float_as_uint(rs), __float_as_uint(rs), false, false); rs = __uint_as_float(rr[0]) + __uint_as_float(rr[1]); }
      l = l * al + rs;
      o = MFMA32(v0, __builtin_bit_cast(bf16x8, w0), o); o = MFMA32(v1, __builtin_bit_cast(bf16x8, w1), o);
    }
  }
  const float sc = gq / l;
  char* op = ws + WS_A + ((size_t)T * KS_A + hd * 2) * 1024 + lane * 16;
  *(u32x4*)op = pack8(o, 0, sc); *(u32x4*)(op + 1024) = pack8(o, 8, sc);
  STAMP_AT(4); STAMP_END();
}
}

namespace k3 {
constexpr int A_OFF = 0, W_OFF = 32768, STAGE = 49152, NSTAGE = 3, LDS_BYTES = NSTAGE * STAGE, NCH = KS_A / 8;
__global__ __launch_bounds__(512, 2) void kernel(const char* wo, const char* wd, char* ws, char* out) {
  extern __shared__ __attribute__((aligned(16))) char smem[];
  LAS char* lds = (LAS char*)smem;
  const int tid = threadIdx.x, lane = tid & 63, wid = __builtin_amdgcn_readfirstlane(tid >> 6);
  const int w = blockIdx.x, xcd = w & 7, i = w >> 3;
  const int tm = (xcd >> 1) * 4 + (i & 3), tn = (xcd & 1) * 8 + (i >> 2);
  STAMP_DECL(2); STAMP_AT(0);
  const int hi = lane >> 5, l31 = lane & 31;
  const int th = wid & 1, kg = wid >> 1;
  const char* asrc = ws + WS_A + (size_t)(tm * 4) * KS_A * 1024;
  const unsigned ldsb = (unsigned)(uintptr_t)lds, l16 = lane * 16;
  unsigned woff[2];
#pragma unroll
  for (int e = 0; e < 2; ++e) { const int row = (wid * 2 + e) * 8 + (lane >> 3), pos = lane & 7, c = pos ^ (((row >> 1) & 1) << 2); woff[e] = (unsigned)(row * 2048 + tn * 128 + c * 16); }
  auto dma = [&](int c, unsigned st) {
    const int ks0 = c < 28 ? KS_O + 8 * c : 8 * (c - 28);
    const char* wsrc = c < 28 ? wd + (size_t)c * 128 * 2048 : wo + (size_t)(c - 28) * 128 * 2048;
#pragma unroll
    for (int tl = 0; tl < 4; ++tl) glds16(asrc + ((size_t)tl * KS_A + ks0 + wid) * 1024, l16, ldsb + st + A_OFF + (tl * 8 + wid) * 1024);
#pragma unroll
    for (int e = 0; e < 2; ++e) glds16(wsrc, woff[e], ldsb + st + W_OFF + (wid * 2 + e) * 1024);
  };
  const int q4 = (lane & 15) >> 2, p4 = lane & 3, h16 = (lane >> 4) & 1;
  const unsigned wa = (unsigned)(W_OFF + (4 * hi + q4) * 128 + ((32 * h16 + 8 * p4) ^ 0));
  const unsigned wsw = (unsigned)(((q4 >> 1) & 1) << 6);
  f32x16 acc[2][2];
#pragma unroll
  for (int a = 0; a < 2; ++a)
#pragma unroll
    for (int b = 0; b < 2; ++b)
#pragma unroll
      for (int e = 0; e < 16; ++e) acc[a][b][e] = 0.f;
  float gt[2];
#pragma unroll
  for (int u = 0; u < 2; ++u) gt[u] = ((const float*)(ws + WS_G))[tm * 128 + th * 64 + u * 32 + l31];
  dma(0, 0); dma(1, STAGE);
  asm volatile("s_waitcnt vmcnt(6)\n\ts_barrier" ::: "memory");
  STAMP_AT(1);
  unsigned st = 0;
  for (int c = 0; c < NCH; ++c) {
    const unsigned st2 = st >= STAGE ? st - STAGE : st + 2 * STAGE;
    dma(c + 2 < NCH ? c + 2 : NCH - 1, st2);
    if (c == 28) {
#pragma unroll
      for (int u = 0; u < 2; ++u)
#pragma unroll
        for (int b = 0; b < 2; ++b)
#pragma unroll
          for (int e = 0; e < 16; ++e) acc[u][b][e] *= (1.f - gt[u]);
    }
#pragma unroll
    for (int s2 = 0; s2 < 2; ++s2) {
      const int ks = kg * 2 + s2;
      bf16x8 af[2], wf[2];
#pragma unroll
      for (int u = 0; u < 2; ++u) af[u] = *(LAS const bf16x8*)(lds + st + A_OFF + ((th * 2 + u) * 8 + ks) * 1024 + lane * 16);
#pragma unroll
      for (int b = 0; b < 2; ++b) { LAS const char* p = lds + st + ((wa + ks * 16 * 128 + b * 64) ^ wsw); wf[b] = tr_frag(p, p + 8 * 128); }
#pragma unroll
      for (int u = 0; u < 2; ++u)
#pragma unroll
        for (int b = 0; b < 2; ++b) acc[u][b] = MFMA32(wf[b], af[u], acc[u][b]);
    }
    asm volatile("s_waitcnt vmcnt(6) lgkmcnt(0)\n\ts_barrier" ::: "memory");
    st = st == 2 * STAGE ? 0 : st + STAGE;
  }
  WAIT_ALL_BAR();
  STAMP_AT(2);
  LAS f32x4* red = (LAS f32x4*)lds;
#pragma unroll
  for (int u = 0; u < 2; ++u)
#pragma unroll
    for (int b = 0; b < 2; ++b)
#pragma unroll
      for (int e = 0; e < 4; ++e) red[((((kg * 2 + th) * 2 + u) * 2 + b) * 4 + e) * 64 + lane] = (f32x4){acc[u][b][4 * e], acc[u][b][4 * e + 1], acc[u][b][4 * e + 2], acc[u][b][4 * e + 3]};
  __syncthreads();
  {
    const int blk = wid;
    f32x4 s[4];
#pragma unroll
    for (int e = 0; e < 4; ++e) { s[e] = red[((0 * 8 + blk) * 4 + e) * 64 + lane];
#pragma unroll
      for (int g = 1; g < 4; ++g) s[e] += red[((g * 8 + blk) * 4 + e) * 64 + lane]; }
    const int tok = tm * 128 + (blk >> 1) * 32 + l31, f0 = tn * 64 + (blk & 1) * 32 + 4 * hi;
    char* op = out + (size_t)tok * 2048 + f0 * 2;
#pragma unroll
    for (int e = 0; e < 4; ++e) { u32x2 v; v[0] = cvtpk(s[e][0], s[e][1]); v[1] = cvtpk(s[e][2], s[e][3]); *(u32x2*)(op + e * 16) = v; }
  }
  STAMP_AT(3); STAMP_END();
}
}

extern "C" void kernel_launch(void* const* d_in, const int* in_sizes, int n_in, void* d_out, int out_size, void* d_ws, size_t ws_size, hipStream_t stream) {
  const char* x = (const char*)d_in[0]; const char* wqkv = (const char*)d_in[1]; const char* wo = (const char*)d_in[2];
  const char* wg = (const char*)d_in[4]; const char* wu = (const char*)d_in[5]; const char* wd = (const char*)d_in[6]; const char* gw = (const char*)d_in[7];
  char* ws = (char*)d_ws;
  (void)hipFuncSetAttribute((const void*)k1::kernel, hipFuncAttributeMaxDynamicSharedMemorySize, k1::LDS_BYTES);
  (void)hipFuncSetAttribute((const void*)k2::kernel, hipFuncAttributeMaxDynamicSharedMemorySize, k2::LDS_BYTES);
  (void)hipFuncSetAttribute((const void*)k3::kernel, hipFuncAttributeMaxDynamicSharedMemorySize, k3::LDS_BYTES);
  k1::kernel<<<256, 512, k1::LDS_BYTES, stream>>>(x, wqkv, wg, wu, gw, ws);
  k2::kernel<<<256, 512, k2::LDS_BYTES, stream>>>(ws);
  k3::kernel<<<256, 512, k3::LDS_BYTES, stream>>>(wo, wd, ws, (char*)d_out);
}
```

```cpp
#include <hip/hip_runtime.h>
#include <stdint.h>

typedef __bf16 bf16x8 __attribute__((ext_vector_type(8)));
typedef __bf16 bf16x2 __attribute__((ext_vector_type(2)));
typedef short s16x4 __attribute__((ext_vector_type(4)));
typedef float f32x16 __attribute__((ext_vector_type(16)));
typedef float f32x4 __attribute__((ext_vector_type(4)));
typedef float f32x2 __attribute__((ext_vector_type(2)));
typedef unsigned u32x4 __attribute__((ext_vector_type(4)));
typedef unsigned u32x2 __attribute__((ext_vector_type(2)));
typedef unsigned long long u64;
#define LAS __attribute__((address_space(3)))
#define GAS __attribute__((address_space(1)))
#define DI __device__ __forceinline__
#define SBAR() __builtin_amdgcn_sched_barrier(0)
#define PIN(x) asm volatile("" : "+v"(x))
#define MFMA32(a, b, c) __builtin_amdgcn_mfma_f32_32x32x16_bf16((a), (b), (c), 0, 0, 0)
#define MFMA16(a, b, c) __builtin_amdgcn_mfma_f32_16x16x32_bf16((a), (b), (c), 0, 0, 0)
#define RLX_AGENT __ATOMIC_RELAXED, __HIP_MEMORY_SCOPE_AGENT
#define SC1 16

constexpr int SEQ = 2048, DM = 1024, FF = 3584, NH = 32;
constexpr int NT32 = SEQ / 32;
constexpr int KS_O = DM / 16;
constexpr int KS_H = FF / 16;
constexpr int KS_A = KS_O + KS_H;
constexpr unsigned WS_E = 0;
constexpr unsigned WS_FKV = 256;
constexpr unsigned WS_FP1 = WS_FKV + 32 * 32 * 8;
constexpr unsigned WS_FO = WS_FP1 + 256 * 8;
constexpr unsigned WS_G = 16384;
constexpr unsigned WS_K = 32768;
constexpr unsigned WS_V = WS_K + NH * NT32 * 2048;
constexpr unsigned WS_A = WS_V + NH * NT32 * 2048;
constexpr unsigned WS_W = WS_A + NT32 * KS_A * 1024;
constexpr unsigned WS_END = WS_W + 16 * KS_A * 2048;
static_assert(WS_FO + 256 * 8 <= WS_G, "flags");

DI unsigned cvtpk(float lo, float hi) { f32x2 v = {lo, hi}; bf16x2 b = __builtin_convertvector(v, bf16x2); return __builtin_bit_cast(unsigned, b); }
DI u32x4 pack8(const f32x16& a, int o, float s) {
  u32x4 w; w[0] = cvtpk(a[o + 0] * s, a[o + 1] * s); w[1] = cvtpk(a[o + 2] * s, a[o + 3] * s); w[2] = cvtpk(a[o + 4] * s, a[o + 5] * s); w[3] = cvtpk(a[o + 6] * s, a[o + 7] * s); return w; }
DI bf16x8 tr_frag(LAS const char* p0, LAS const char* p1) {
  s16x4 lo = __builtin_amdgcn_ds_read_tr16_b64_v4i16((LAS s16x4*)p0);
  s16x4 hi = __builtin_amdgcn_ds_read_tr16_b64_v4i16((LAS s16x4*)p1);
  return __builtin_bit_cast(bf16x8, __builtin_shufflevector(lo, hi, 0, 1, 2, 3, 4, 5, 6, 7)); }
DI void glds16(const char* sbase, unsigned voff, unsigned ldst) {
  asm volatile("s_mov_b32 m0, %0\n\ts_nop 0\n\tglobal_load_lds_dwordx4 %1, %2" ::"s"(ldst), "v"(voff), "s"(sbase) : "memory"); }
DI void glds16c(int on, const char* sbase, unsigned voff, unsigned ldst) {
  asm volatile("s_cmp_lt_i32 %3, 1\n\ts_cbranch_scc1 1f\n\ts_mov_b32 m0, %0\n\ts_nop 0\n\tglobal_load_lds_dwordx4 %1, %2\n1:" ::"s"(ldst), "v"(voff), "s"(sbase), "s"(on) : "memory", "scc"); }
DI void l2_prefetch(const char* sbase, unsigned voff, unsigned ldst) {
  asm volatile("s_mov_b32 m0, %0\n\ts_nop 0\n\tglobal_load_lds_dword %1, %2 sc1" ::"s"(ldst), "v"(voff), "s"(sbase) : "memory"); }
#define STAMP_DECL(kid)
#define STAMP_AT(i)
#define STAMP_END()
#define WAIT_VM(n) asm volatile("s_waitcnt vmcnt(" #n ")" ::: "memory")
#define WAITVM_BAR(n) asm volatile("s_waitcnt vmcnt(" #n ") lgkmcnt(0)\n\ts_barrier" ::: "memory")

typedef GAS u64 gu64;
DI void flag_set(char* ws, unsigned off, u64 epoch) { __hip_atomic_store((gu64*)(ws + off), epoch, RLX_AGENT); }
DI void flags_wait(char* ws, unsigned off, int n, u64 epoch, int lane) {
  gu64* p = (gu64*)(ws + off) + (lane < n ? lane : 0);
  for (unsigned spins = 0; spins < (1u << 16); ++spins) {
    const u64 v = __hip_atomic_load(p, RLX_AGENT);
    if (__all(v == epoch)) break;
    __builtin_amdgcn_s_sleep(2);
  }
  asm volatile("" ::: "memory");
}
DI void st16(__amdgpu_buffer_rsrc_t rs, unsigned voff, unsigned soff, u32x4 v) { __builtin_amdgcn_raw_buffer_store_b128(v, rs, voff + soff, 0, SC1); }

namespace lm {
constexpr int P1_X = 0, P1_WQ = 32768, P1_WG = 45056, P1_WU = 59520, P1_STAGE = 73984, P1_PF = 2 * P1_STAGE;
constexpr int AT_STAGE = 32768, AT_WT = 3 * AT_STAGE, AT_Q = 139264;
constexpr int P3_G = 131072, BYTES = 163840;
}

constexpr float QSCALE = 0.17677669529663687f * 1.4426950408889634f;
template <int WC> DI u64 phase1(LAS char* lds, const char* x, const char* wqkv, const char* wo, const char* wg, const char* wu, const char* wd, const char* gatew, char* ws, u64 e0, int w, int j, int r, int wid, int lane) {
  using namespace lm;
  STAMP_DECL(0); STAMP_AT(0);
  const int wr = wid & 3, hi = lane >> 5, l31 = lane & 31;
  const unsigned ldsb = (unsigned)(uintptr_t)lds;
  const char* xsrc = x + (size_t)(r * 256 + wid * 32) * 2048;
  unsigned xoff[2];
#pragma unroll
  for (int e = 0; e < 2; ++e) xoff[e] = (unsigned)((lane >> 3) * 2048 + (((lane & 7) ^ ((4 * e + (lane >> 4)) & 7)) << 4));
  const char* wsrc[5]; unsigned woff[5], wdst[5], wstep[5];
#pragma unroll
  for (int m = 0; m < 5; ++m) {
    const int p = wid + 8 * m; const bool isq = p < 12, isg = p < 26;
    const int pp = isq ? p : (isg ? p - 12 : p - 26);
    wsrc[m] = isq ? wqkv : (isg ? wg : wu);
    wdst[m] = (unsigned)__builtin_amdgcn_readfirstlane((isq ? P1_WQ : (isg ? P1_WG : P1_WU)) + pp * 1024);
    wstep[m] = isq ? 64 * 6144 : 64 * 7168;
    const int cc = 64 * pp + lane, rq = cc / 12, pq = cc % 12, rg = cc / 14, pg = cc % 14;
    woff[m] = isq ? (unsigned)(rq * 6144 + ((pq >> 2) * 1024 + j * 32 + (pq & 3) * 8) * 2) : (unsigned)(rg * 7168 + (j * 112 + pg * 8) * 2);
  }
#define P1_DMA_X(on, q, t, st) glds16c(on, xsrc + (size_t)(t) * 128 + (q) * 8 * 2048, xoff[(q) & 1], ldsb + (st) + P1_X + (wid * 4 + (q)) * 1024)
#define P1_DMA_W(on, m, t, st) glds16c(on, wsrc[m] + (size_t)(t) * wstep[m], woff[m], ldsb + (st) + wdst[m])
#pragma unroll
  for (int q = 0; q < 4; ++q) P1_DMA_X(1, q, 0, 0);
#pragma unroll
  for (int m = 0; m < 5; ++m) P1_DMA_W(1, m, 0, 0);
  asm volatile("s_barrier" ::: "memory");
#pragma unroll
  for (int q = 0; q < 4; ++q) P1_DMA_X(1, q, 1, P1_STAGE);
#pragma unroll
  for (int m = 0; m < 5; ++m) P1_DMA_W(1, m, 1, P1_STAGE);
  unsigned pfoff, pfstep;
  {
    const int jj = j & 3, xg = j >> 2, row = (r * 4 + jj) * 2 + (wid & 1), l6 = lane % 6, l7 = (lane & 31) % 7;
    const unsigned ox = (unsigned)((r * 256 + jj * 64 + wid * 16 + (lane & 15)) * 2048);
    const unsigned oq = (unsigned)(row * 6144 + (l6 >> 1) * 2048 + xg * 256 + (l6 & 1) * 128), og = (unsigned)(row * 7168 + xg * 896 + l7 * 128);
    pfoff = wid < 4 ? ox : (wid < 6 ? oq : og); pfstep = wid < 4 ? 128 : (wid < 6 ? 64 * 6144 : 64 * 7168);
  }
  const char* pfl = (wid >= 6 ? (lane < 32 ? wg : wu) : (wid < 4 ? x : wqkv)) + pfoff;
  auto pf = [&](int t) { asm volatile("s_mov_b32 m0, %0\n\ts_nop 0\n\tglobal_load_lds_dword %1, off sc1" ::"s"(ldsb + P1_PF), "v"(pfl + (size_t)t * pfstep) : "memory"); };
  auto pf_w = [&]() {
    const int li = wid * 64 + lane;
    const int f = 36 * w + 2 * (li < 288 ? li >> 4 : 0), ks = f >> 5, fb = f & 31;
    const char* src = (ks < KS_O ? wo + (size_t)ks * 16 * 2048 : wd + (size_t)(ks - KS_O) * 16 * 2048) + fb * 64 + (li & 15) * 2048;
    asm volatile("s_mov_b32 m0, %0\n\ts_nop 0\n\tglobal_load_lds_dword %1, off sc1" ::"s"(ldsb + P1_PF), "v"(src) : "memory"); };
  unsigned xa[4];
#pragma unroll
  for (int ks = 0; ks < 4; ++ks) xa[ks] = (unsigned)(P1_X + (wr * 64 + l31) * 128 + (((2 * ks + hi) ^ ((l31 >> 1) & 7)) << 4));
  const int q4 = (lane & 15) >> 2, p4 = lane & 3, h16 = (lane >> 4) & 1;
  const unsigned wqa = (unsigned)(P1_WQ + (8 * hi + q4) * 192 + 32 * h16 + 8 * p4);
  const unsigned wga = (unsigned)((h16 ? P1_WU : P1_WG) + (8 * hi + q4) * 224 + 8 * p4);
  f32x16 acc[2][5];
#pragma unroll
  for (int u = 0; u < 2; ++u)
#pragma unroll
    for (int b = 0; b < 5; ++b)
#pragma unroll
      for (int i = 0; i < 16; ++i) acc[u][b][i] = 0.f;
  bf16x8 xf[2][2], wf[2][5];
  auto ldx = [&](int set, int u, unsigned st, int ks) { xf[set][u] = *(LAS const bf16x8*)(lds + st + xa[ks] + u * 4096); };
  auto ldw = [&](int set, int b, unsigned st, int ks) {
    if (WC == 0 && b < 3) { LAS const char* p = lds + st + wqa + ks * 16 * 192 + b * 64; wf[set][b] = tr_frag(p, p + 4 * 192); }
    else { const int gb = WC == 0 ? b - 3 : b + 2; LAS const char* p = lds + st + wga + ks * 16 * 224 + gb * 32; wf[set][b] = tr_frag(p, p + 4 * 224); } };
  auto mma = [&](int set, int u, int b) {
    if (WC == 0 && b == 2) acc[u][b] = MFMA32(xf[set][u], wf[set][b], acc[u][b]); else acc[u][b] = MFMA32(wf[set][b], xf[set][u], acc[u][b]); };
#define P1_STEP(cs, nst, nks, DMA0, DMA1, DMA2, DMA3, DMA4) do { \
    ldx((cs) ^ 1, 0, nst, nks); ldx((cs) ^ 1, 1, nst, nks); mma(cs, 0, 0); DMA0; SBAR(); \
    ldw((cs) ^ 1, 0, nst, nks); mma(cs, 1, 0); SBAR(); mma(cs, 0, 1); DMA1; SBAR(); \
    ldw((cs) ^ 1, 1, nst, nks); mma(cs, 1, 1); SBAR(); mma(cs, 0, 2); DMA2; SBAR(); \
    ldw((cs) ^ 1, 2, nst, nks); mma(cs, 1, 2); SBAR(); mma(cs, 0, 3); DMA3; SBAR(); \
    ldw((cs) ^ 1, 3, nst, nks); mma(cs, 1, 3); SBAR(); ldw((cs) ^ 1, 4, nst, nks); mma(cs, 0, 4); DMA4; SBAR(); \
    mma(cs, 1, 4); SBAR(); } while (0)
#define NOP (void)0
  pf(2); pf(3); pf_w();
  asm volatile("s_waitcnt vmcnt(12)\n\ts_barrier" ::: "memory");
  STAMP_AT(1);
  ldx(0, 0, 0, 0); ldx(0, 1, 0, 0);
#pragma unroll
  for (int b = 0; b < 5; ++b) ldw(0, b, 0, 0);
  unsigned cur = 0;
#pragma unroll 1
  for (int t = 0; t < 16; ++t) {
    const unsigned nxt = P1_STAGE - cur;
    const int m1 = t * (15 - t), m2 = 14 - t;
    const int tp = t + 4 < 16 ? t + 4 : 15;
    P1_STEP(0, cur, 1, P1_DMA_W(m1, 1, t + 1, nxt), P1_DMA_W(m1, 2, t + 1, nxt), P1_DMA_W(m1, 3, t + 1, nxt), P1_DMA_W(m1, 4, t + 1, nxt), pf(tp));
    P1_STEP(1, cur, 2, NOP, NOP, NOP, NOP, NOP);
    P1_STEP(0, cur, 3, NOP, NOP, NOP, NOP, NOP);
    WAITVM_BAR(1);
    P1_STEP(1, nxt, 0, P1_DMA_X(m2, 0, t + 2, cur), P1_DMA_X(m2, 1, t + 2, cur), P1_DMA_X(m2, 2, t + 2, cur), P1_DMA_X(m2, 3, t + 2, cur), P1_DMA_W(m2, 0, t + 2, cur));
    cur = nxt;
  }
  WAITVM_BAR(0);
  STAMP_AT(2);
  const u64 epoch = (((u64)(unsigned)__builtin_amdgcn_readfirstlane((unsigned)(e0 >> 32)) << 32) | (unsigned)__builtin_amdgcn_readfirstlane((unsigned)e0)) + 1;
  const __amdgpu_buffer_rsrc_t rs = __builtin_amdgcn_make_buffer_rsrc(ws, 0, WS_END, 0x00020000);
  const unsigned l16 = lane * 16;
  const int gtok = r * 256 + j * 8 + wid;
  const u32x4* gxp = (const u32x4*)(x + (size_t)gtok * 2048) + lane * 2; const u32x4* ggp = (const u32x4*)gatew + lane * 2;
  const u32x4 ga0 = gxp[0], ga1 = gxp[1], gb0 = ggp[0], gb1 = ggp[1];
  SBAR();
  if (WC == 0) {
#pragma unroll
    for (int u = 0; u < 2; ++u) {
      const int tl = wr * 2 + u, T = r * 8 + tl;
      const u32x4 k0 = pack8(acc[u][1], 0, 1.f), k1 = pack8(acc[u][1], 8, 1.f), v0 = pack8(acc[u][2], 0, 1.f), v1 = pack8(acc[u][2], 8, 1.f);
      const unsigned go = (unsigned)(j * NT32 + T) * 2048;
      st16(rs, l16, WS_K + go, k0); st16(rs, l16, WS_K + go + 1024, k1); st16(rs, l16, WS_V + go, v0); st16(rs, l16, WS_V + go + 1024, v1);
      LAS char* kl = lds + tl * 2048 + l16;
      *(LAS u32x4*)(kl) = k0; *(LAS u32x4*)(kl + 1024) = k1; *(LAS u32x4*)(kl + 16384) = v0; *(LAS u32x4*)(kl + 16384 + 1024) = v1;
      LAS char* ql = lds + AT_Q + tl * 2048 + l16;
      *(LAS u32x4*)(ql) = pack8(acc[u][0], 0, QSCALE); *(LAS u32x4*)(ql + 1024) = pack8(acc[u][0], 8, QSCALE);
    }
    SBAR();
    const unsigned wl = (unsigned)((lane >> 2) * 2048 + (lane & 3) * 16);
#pragma unroll
    for (int m = 0; m < 9; ++m) { const int f = 36 * w + 9 * wr + m, ks = f >> 5, fb = f & 31;
      const char* src = (ks < KS_O ? wo + (size_t)ks * 16 * 2048 : wd + (size_t)(ks - KS_O) * 16 * 2048) + fb * 64;
      glds16(src, wl, ldsb + AT_WT + (9 * wr + m) * 1024); }
    SBAR();
  }
#pragma unroll
  for (int u = 0; u < 2; ++u) {
    const int T = r * 8 + wr * 2 + u;
#pragma unroll
    for (int b = (WC == 0 ? 3 : 0); b < 5; ++b) {
      const int gb = WC == 0 ? b - 3 : b + 2;
      float h[8];
#pragma unroll
      for (int i = 0; i < 8; ++i) { const float g = acc[u][b][i], up = acc[u][b][8 + i];
        h[i] = g * __builtin_amdgcn_rcpf(1.f + __builtin_amdgcn_exp2f(-1.4426950408889634f * g)) * up; }
      u32x4 hw; hw[0] = cvtpk(h[0], h[1]); hw[1] = cvtpk(h[2], h[3]); hw[2] = cvtpk(h[4], h[5]); hw[3] = cvtpk(h[6], h[7]);
      st16(rs, l16, WS_A + (unsigned)(T * KS_A + KS_O + j * 7 + gb) * 1024, hw);
    }
  }
  SBAR();
  if (WC == 0) {
    WAIT_VM(4);
    if (lane == 0) flag_set(ws, WS_FKV + (unsigned)((j * 8 + r) * 4 + wr) * 8, epoch);
    LAS const char* tp = lds + AT_WT + (4 * hi + q4) * 64 + 32 * h16 + 8 * p4;
#pragma unroll
    for (int m = 0; m < 9; ++m) { const int f = 36 * w + 9 * wr + m, ks = f >> 5, fb = f & 31;
      const bf16x8 v = tr_frag(tp + (9 * wr + m) * 1024, tp + (9 * wr + m) * 1024 + 8 * 64);
      st16(rs, l16, WS_W + (unsigned)(((fb >> 1) * KS_A + ks) * 2 + (fb & 1)) * 1024, __builtin_bit_cast(u32x4, v)); }
  }
  {
    float s = 0.f;
#pragma unroll
    for (int e = 0; e < 4; ++e) { const unsigned ua0 = ga0[e], ub0 = gb0[e], ua1 = ga1[e], ub1 = gb1[e];
      s = __builtin_amdgcn_fdot2_f32_bf16(__builtin_bit_cast(bf16x2, ua0), __builtin_bit_cast(bf16x2, ub0), s, false);
      s = __builtin_amdgcn_fdot2_f32_bf16(__builtin_bit_cast(bf16x2, ua1), __builtin_bit_cast(bf16x2, ub1), s, false); }
#pragma unroll
    for (int o = 32; o >= 1; o >>= 1) s += __shfl_xor(s, o);
    const float g = __builtin_amdgcn_rcpf(1.f + __builtin_amdgcn_exp2f(-1.4426950408889634f * s));
    if (lane == 0) __hip_atomic_store((GAS unsigned*)(ws + WS_G + gtok * 4), __float_as_uint(g), RLX_AGENT);
  }
  STAMP_AT(3); STAMP_END();
  return epoch;
}

DI float halfmax(float m) { auto rr = __builtin_amdgcn_permlane32_swap(__float_as_uint(m), __float_as_uint(m), false, false); return fmaxf(__uint_as_float(rr[0]), __uint_as_float(rr[1])); }
DI void phase2(LAS char* lds, char* ws, u64 epoch, int hd, int r, int wid, int lane) {
  using namespace lm;
  constexpr int NTILE = 8, STAGE = AT_STAGE;
  STAMP_DECL(1); STAMP_AT(0);
  const int T = r * 8 + wid;
  const char* kbase = ws + WS_K + (size_t)hd * NT32 * 2048; const char* vbase = ws + WS_V + (size_t)hd * NT32 * 2048;
  const unsigned ldsb = (unsigned)(uintptr_t)lds, l16 = lane * 16;
#define P2_DMA1(on, e, t, st) glds16c(on, ((e) < 2 ? kbase : vbase) + (size_t)((r + (t)) & 7) * 16384 + wid * 2048 + ((e) & 1) * 1024, l16, ldsb + (st) + ((e) >> 1) * 16384 + wid * 2048 + ((e) & 1) * 1024)
  if (wid == 0) { flags_wait(ws, WS_FKV + (unsigned)hd * 256, 32, epoch, lane); __builtin_amdgcn_fence(__ATOMIC_ACQUIRE, "agent"); WAIT_VM(0); }
  asm volatile("s_barrier" ::: "memory");
  STAMP_AT(1);
  P2_DMA1(1, 0, 1, STAGE); P2_DMA1(1, 2, 1, STAGE);
  LAS const char* lp = lds + lane * 16;
  const bf16x8 q0 = *(LAS const bf16x8*)(lp + AT_Q + wid * 2048), q1 = *(LAS const bf16x8*)(lp + AT_Q + wid * 2048 + 1024);
#define KFR(st, b, h) (*(LAS const bf16x8*)(lp + (st) + (b) * 2048 + (h) * 1024))
#define VFR(st, b, h) (*(LAS const bf16x8*)(lp + (st) + 16384 + (b) * 2048 + (h) * 1024))
  f32x16 negm;
  {
    float m = -INFINITY;
#pragma unroll
    for (int b = 0; b < 4; ++b) {
      f32x16 s = {}; s = MFMA32(KFR(0, b, 0), q0, s); s = MFMA32(KFR(0, b, 1), q1, s);
#pragma unroll
      for (int e = 0; e < 16; ++e) m = fmaxf(m, s[e]);
    }
    m = halfmax(m);
#pragma unroll
    for (int e = 0; e < 16; ++e) negm[e] = -m;
    PIN(negm);
  }
  bf16x8 ones; { const bool on = ((lane & 1) == ((lane >> 4) & 1)); const __bf16 o = on ? (__bf16)1.0f : (__bf16)0.0f;
#pragma unroll
    for (int e = 0; e < 8; ++e) ones[e] = o; }
  PIN(ones);
  f32x16 o = {}; f32x4 ls0 = {}, ls1 = {};
  f32x16 sA, sB; u32x4 pA0, pA1, pB0 = {}, pB1 = {}; bf16x8 kA0, kA1, kB0, kB1, vA0, vA1, vB0 = {}, vB1 = {};
  sA = MFMA32(KFR(0, 0, 0), q0, negm); sA = MFMA32(KFR(0, 0, 1), q1, sA);
  kB0 = KFR(0, 1, 0); kB1 = KFR(0, 1, 1);
  STAMP_AT(2);
#define EX(S, i) S[i] = __builtin_amdgcn_exp2f(S[i])
#define PK(S, i) cvtpk(S[i], S[i + 1])
#define BF(x) __builtin_bit_cast(bf16x8, x)
#define P2_STEP(SX, SY, PX0, PX1, PY0, PY1, KX0, KX1, KY0, KY1, VX0, VX1, VY0, VY1, kst, kb, vst, vb, DMA) do { \
    SY = MFMA32(KY0, q0, negm); SBAR(); VX0 = VFR(vst, vb, 0); EX(SX, 0); EX(SX, 1); EX(SX, 2); EX(SX, 3); PIN(SX); SBAR(); \
    o = MFMA32(VY0, BF(PY0), o); SBAR(); VX1 = VFR(vst, vb, 1); EX(SX, 4); EX(SX, 5); EX(SX, 6); PX0[0] = PK(SX, 0); PX0[1] = PK(SX, 2); PIN(SX); PIN(PX0); SBAR(); \
    ls0 = MFMA16(ones, BF(PY0), ls0); SBAR(); EX(SX, 7); PX0[2] = PK(SX, 4); SBAR(); PX0[3] = PK(SX, 6); PIN(SX); PIN(PX0); SBAR(); \
    SY = MFMA32(KY1, q1, SY); SBAR(); KX0 = KFR(kst, kb, 0); EX(SX, 8); EX(SX, 9); EX(SX, 10); EX(SX, 11); PIN(SX); SBAR(); \
    o = MFMA32(VY1, BF(PY1), o); SBAR(); KX1 = KFR(kst, kb, 1); EX(SX, 12); EX(SX, 13); EX(SX, 14); PX1[0] = PK(SX, 8); PX1[1] = PK(SX, 10); PIN(SX); PIN(PX1); SBAR(); \
    ls1 = MFMA16(ones, BF(PY1), ls1); SBAR(); EX(SX, 15); PX1[2] = PK(SX, 12); SBAR(); PX1[3] = PK(SX, 14); PIN(PX1); DMA; SBAR(); } while (0)
#define STEP_A(kst, kb, vst, vb, DMA) P2_STEP(sA, sB, pA0, pA1, pB0, pB1, kA0, kA1, kB0, kB1, vA0, vA1, vB0, vB1, kst, kb, vst, vb, DMA)
#define STEP_B(kst, kb, vst, vb, DMA) P2_STEP(sB, sA, pB0, pB1, pA0, pA1, kB0, kB1, kA0, kA1, vB0, vB1, vA0, vA1, kst, kb, vst, vb, DMA)
  unsigned st = 0;
#pragma unroll 1
  for (int t = 0; t < NTILE; ++t) {
    const unsigned sn = st == 2 * STAGE ? 0 : st + STAGE, sp = st == 0 ? 2 * STAGE : st - STAGE;
    const int a1 = NTILE - 1 - t, a2 = NTILE - 2 - t;
    STEP_A(st, 2, st, 0, P2_DMA1(a1, 1, t + 1, sn)); STEP_B(st, 3, st, 1, P2_DMA1(a1, 3, t + 1, sn));
    STEP_A(st, 4, st, 2, NOP); STEP_B(st, 5, st, 3, NOP); STEP_A(st, 6, st, 4, NOP); STEP_B(st, 7, st, 5, NOP);
    asm volatile("s_waitcnt vmcnt(0)\n\ts_barrier" ::: "memory");
    STEP_A(sn, 0, st, 6, P2_DMA1(a2, 0, t + 2, sp)); STEP_B(sn, 1, st, 7, P2_DMA1(a2, 2, t + 2, sp));
    st = sn;
  }
  o = MFMA32(vB0, BF(pB0), o); o = MFMA32(vB1, BF(pB1), o);
  ls0 = MFMA16(ones, BF(pB0), ls0); ls1 = MFMA16(ones, BF(pB1), ls1);
  STAMP_AT(3);
  {
    gu64* p = (gu64*)(ws + WS_FP1) + (8 * (4 * r + (lane & 3)) + ((lane >> 2) & 7));
    for (unsigned spins = 0; spins < (1u << 16); ++spins) { const u64 v = __hip_atomic_load(p, RLX_AGENT); if (__all(v == epoch)) break; __builtin_amdgcn_s_sleep(2); }
    asm volatile("" ::: "memory");
  }
  const float gq = __uint_as_float(__hip_atomic_load((GAS unsigned*)(ws + WS_G + (T * 32 + (lane & 31)) * 4), RLX_AGENT));
  float l = (lane & 16) ? ls0[1] + ls1[1] : ls0[0] + ls1[0];
  float amax = 0.f;
#pragma unroll
  for (int e = 0; e < 16; ++e) amax = fmaxf(amax, fabsf(o[e]));
  const bool bad = !(l < 1.0995116e12f) || !(amax < INFINITY);
  if (__builtin_expect(__any(bad), 0)) {
    const __amdgpu_buffer_rsrc_t rk = __builtin_amdgcn_make_buffer_rsrc(ws, 0, WS_END, 0x00020000);
    float m = -INFINITY; l = 0.f;
#pragma unroll
    for (int e = 0; e < 16; ++e) o[e] = 0.f;
    for (int b = 0; b < NT32; ++b) {
      const unsigned ko = WS_K + (unsigned)(hd * NT32 + b) * 2048, vo = WS_V + (unsigned)(hd * NT32 + b) * 2048;
      const bf16x8 k0 = BF(__builtin_amdgcn_raw_buffer_load_b128(rk, l16, ko, SC1)), k1 = BF(__builtin_amdgcn_raw_buffer_load_b128(rk, l16, ko + 1024, SC1));
      const bf16x8 v0 = BF(__builtin_amdgcn_raw_buffer_load_b128(rk, l16, vo, SC1)), v1 = BF(__builtin_amdgcn_raw_buffer_load_b128(rk, l16, vo + 1024, SC1));
      f32x16 s = {}; s = MFMA32(k0, q0, s); s = MFMA32(k1, q1, s);
      float mb = s[0];
#pragma unroll
      for (int e = 1; e < 16; ++e) mb = fmaxf(mb, s[e]);
      const float mn = fmaxf(m, halfmax(mb)); const float al = __builtin_amdgcn_exp2f(m - mn); m = mn;
#pragma unroll
      for (int e = 0; e < 16; ++e) { s[e] = __builtin_amdgcn_exp2f(s[e] - mn); o[e] *= al; }
      const u32x4 w0 = pack8(s, 0, 1.f), w1 = pack8(s, 8, 1.f);
      float rsum = 0.f;
#pragma unroll
      for (int e = 0; e < 4; ++e) rsum += __uint_as_float(w0[e] << 16) + __uint_as_float(w0[e] & 0xffff0000u) + __uint_as_float(w1[e] << 16) + __uint_as_float(w1[e] & 0xffff0000u);
      { auto rr = __builtin_amdgcn_permlane32_swap(__float_as_uint(rsum), __float_as_uint(rsum), false, false); rsum = __uint_as_float(rr[0]) + __uint_as_float(rr[1]); }
      l = l * al + rsum;
      o = MFMA32(v0, BF(w0), o); o = MFMA32(v1, BF(w1), o);
    }
  }
  const float sc = gq / l;
  const __amdgpu_buffer_rsrc_t rs = __builtin_amdgcn_make_buffer_rsrc(ws, 0, WS_END, 0x00020000);
  const unsigned oo = WS_A + (unsigned)(T * KS_A + hd * 2) * 1024;
  st16(rs, l16, oo, pack8(o, 0, sc)); st16(rs, l16, oo + 1024, pack8(o, 8, sc));
  WAITVM_BAR(0);
  if (threadIdx.x == 0) flag_set(ws, WS_FO + (unsigned)(r * 32 + hd) * 8, epoch);
  STAMP_AT(4); STAMP_END();
}

DI void phase3(LAS char* lds, char* ws, char* out, u64 epoch, int xcd, int i, int wid, int lane) {
  using namespace lm;
  constexpr int D = 4, NSTEP = KS_A / 8;
  const int tm = (xcd >> 1) * 4 + (i & 3), tn = (xcd & 1) * 8 + (i >> 2);
  STAMP_DECL(2); STAMP_AT(0);
  const int l31 = lane & 31;
#pragma unroll
  for (int q = 0; q < 4; ++q) flags_wait(ws, WS_FP1 + q * 512, 64, epoch, lane);
  const __amdgpu_buffer_rsrc_t rs = __builtin_amdgcn_make_buffer_rsrc(ws, 0, WS_END, 0x00020000);
  const unsigned ab = WS_A + (unsigned)(tm * 4) * KS_A * 1024, wb = WS_W + (unsigned)tn * KS_A * 2048;
  const unsigned l16 = lane * 16;
  bf16x8 a[D][4], wf[D][2];
  auto load = [&](int slot, int s) {
    const unsigned ks = (unsigned)((s < 28 ? KS_O + 8 * s : 8 * (s - 28)) + wid);
#pragma unroll
    for (int u = 0; u < 4; ++u) a[slot][u] = __builtin_bit_cast(bf16x8, __builtin_amdgcn_raw_buffer_load_b128(rs, l16, ab + (u * KS_A + ks) * 1024, SC1));
#pragma unroll
    for (int b = 0; b < 2; ++b) wf[slot][b] = __builtin_bit_cast(bf16x8, __builtin_amdgcn_raw_buffer_load_b128(rs, l16, wb + ks * 2048 + b * 1024, SC1));
  };
  const u64 g2 = __hip_atomic_load((gu64*)(ws + WS_G + (tm * 128 + lane * 2) * 4), RLX_AGENT);
#pragma unroll
  for (int s = 0; s < D; ++s) load(s, s);
  LAS float* gl = (LAS float*)(lds + P3_G + wid * 512);
  *(LAS f32x2*)(gl + lane * 2) = (f32x2){1.f - __uint_as_float((unsigned)g2), 1.f - __uint_as_float((unsigned)(g2 >> 32))};
  f32x16 acc[4][2];
#pragma unroll
  for (int u = 0; u < 4; ++u)
#pragma unroll
    for (int b = 0; b < 2; ++b)
#pragma unroll
      for (int e = 0; e < 16; ++e) acc[u][b][e] = 0.f;
  STAMP_AT(1);
  auto mma = [&](int d) {
#pragma unroll
    for (int u = 0; u < 4; ++u)
#pragma unroll
      for (int b = 0; b < 2; ++b) acc[u][b] = MFMA32(wf[d][b], a[d][u], acc[u][b]); };
#pragma unroll 1
  for (int so = 0; so < 28 - D; so += D) {
#pragma unroll
    for (int d = 0; d < D; ++d) { mma(d); SBAR(); load(d, so + d + D); SBAR(); }
  }
  flags_wait(ws, WS_FO + (unsigned)(tm >> 1) * 256, 32, epoch, lane);
#pragma unroll
  for (int d = 0; d < D; ++d) { mma(d); SBAR(); load(d, 28 + d); SBAR(); }
#pragma unroll
  for (int u = 0; u < 4; ++u) { const float gt = gl[u * 32 + l31];
#pragma unroll
    for (int b = 0; b < 2; ++b)
#pragma unroll
      for (int e = 0; e < 16; ++e) acc[u][b][e] *= gt;
    SBAR(); }
#pragma unroll
  for (int d = 0; d < D; ++d) { mma(d); SBAR(); load(d, 32 + d); SBAR(); }
#pragma unroll
  for (int d = 0; d < D; ++d) mma(d);
  STAMP_AT(2);
  int tz = threadIdx.x; asm volatile("" : "+v"(tz));
  const int ln = tz & 63;
  LAS f32x4* red = (LAS f32x4*)lds + ln;
  f32x4 res[4];
#pragma unroll
  for (int ph = 0; ph < 2; ++ph) {
    if (ph) __syncthreads();
#pragma unroll
    for (int u = 0; u < 4; ++u)
#pragma unroll
      for (int b = 0; b < 2; ++b)
#pragma unroll
        for (int e = 0; e < 2; ++e) { const int o = 8 * ph + 4 * e;
          red[((wid * 8 + u * 2 + b) * 2 + e) * 64] = (f32x4){acc[u][b][o], acc[u][b][o + 1], acc[u][b][o + 2], acc[u][b][o + 3]}; }
    __syncthreads();
    if (ph == 0) STAMP_AT(3);
#pragma unroll
    for (int e = 0; e < 2; ++e) { f32x4 s = red[((0 * 8 + wid) * 2 + e) * 64];
#pragma unroll
      for (int g = 1; g < 8; ++g) s += red[((g * 8 + wid) * 2 + e) * 64];
      res[2 * ph + e] = s; }
  }
  {
    const int tok = tm * 128 + (wid >> 1) * 32 + (ln & 31), f0 = tn * 64 + (wid & 1) * 32 + 4 * (ln >> 5);
    char* op = out + (size_t)tok * 2048 + f0 * 2;
#pragma unroll
    for (int e = 0; e < 4; ++e) { u32x2 v; v[0] = cvtpk(res[e][0], res[e][1]); v[1] = cvtpk(res[e][2], res[e][3]); *(u32x2*)(op + e * 16) = v; }
  }
  STAMP_AT(4); STAMP_END();
}

__global__ __launch_bounds__(512, 2) void mega(const char* x, const char* wqkv, const char* wo, const char* wg, const char* wu, const char* wd, const char* gatew, char* ws, char* out) {
  extern __shared__ __attribute__((aligned(16))) char smem[];
  LAS char* lds = (LAS char*)smem;
  const int tid = threadIdx.x, lane = tid & 63, wid = __builtin_amdgcn_readfirstlane(tid >> 6);
  const int w = blockIdx.x, xcd = w & 7, i = w >> 3, j = xcd * 4 + (i & 3), r = i >> 2;
  const u64 e0 = __hip_atomic_load((gu64*)(ws + WS_E), RLX_AGENT);
  u64 epoch;
  if (wid < 4) epoch = phase1<0>(lds, x, wqkv, wo, wg, wu, wd, gatew, ws, e0, w, j, r, wid, lane); else epoch = phase1<1>(lds, x, wqkv, wo, wg, wu, wd, gatew, ws, e0, w, j, r, wid, lane);
  WAITVM_BAR(0);
  if (tid == 0) flag_set(ws, WS_FP1 + (unsigned)w * 8, epoch);
  phase2(lds, ws, epoch, j, r, wid, lane);
  phase3(lds, ws, out, epoch, xcd, i, wid, lane);
  if (w == 0 && tid == 0) flag_set(ws, WS_E, epoch);
}

extern "C" void kernel_launch(void* const* d_in, const int* in_sizes, int n_in, void* d_out, int out_size, void* d_ws, size_t ws_size, hipStream_t stream) {
  (void)hipFuncSetAttribute((const void*)mega, hipFuncAttributeMaxDynamicSharedMemorySize, lm::BYTES);
  mega<<<256, 512, lm::BYTES, stream>>>((const char*)d_in[0], (const char*)d_in[1], (const char*)d_in[2], (const char*)d_in[4], (const char*)d_in[5], (const char*)d_in[6],
                                        (const char*)d_in[7], (char*)d_ws, (char*)d_out);
}
```

```cpp
#include <hip/hip_runtime.h>
#include <stdint.h>

typedef __bf16 bf16x8 __attribute__((ext_vector_type(8)));
typedef __bf16 bf16x2 __attribute__((ext_vector_type(2)));
typedef short s16x4 __attribute__((ext_vector_type(4)));
typedef float f32x16 __attribute__((ext_vector_type(16)));
typedef float f32x4 __attribute__((ext_vector_type(4)));
typedef float f32x2 __attribute__((ext_vector_type(2)));
typedef unsigned u32x4 __attribute__((ext_vector_type(4)));
typedef unsigned u32x2 __attribute__((ext_vector_type(2)));
typedef unsigned long long u64;
#define LAS __attribute__((address_space(3)))
#define GAS __attribute__((address_space(1)))
#define DI __device__ __forceinline__
#define SBAR() __builtin_amdgcn_sched_barrier(0)
#define PIN(x) asm volatile("" : "+v"(x))
#define MFMA32(a, b, c) __builtin_amdgcn_mfma_f32_32x32x16_bf16((a), (b), (c), 0, 0, 0)
#define MFMA16(a, b, c) __builtin_amdgcn_mfma_f32_16x16x32_bf16((a), (b), (c), 0, 0, 0)
#define RLX_AGENT __ATOMIC_RELAXED, __HIP_MEMORY_SCOPE_AGENT
#define SC1 16

constexpr int SEQ = 2048, DM = 1024, FF = 3584, NH = 32;
constexpr int NT32 = SEQ / 32;
constexpr int KS_O = DM / 16;
constexpr int KS_H = FF / 16;
constexpr int KS_A = KS_O + KS_H;
constexpr unsigned WS_E = 0;
constexpr unsigned WS_FKV = 256;
constexpr unsigned WS_FP1 = WS_FKV + 32 * 32 * 8;
constexpr unsigned WS_FW = WS_FP1 + 256 * 8;
constexpr unsigned WS_FO = WS_FW + 256 * 8;
constexpr unsigned WS_G = 16384;
constexpr unsigned WS_K = 32768;
constexpr unsigned WS_V = WS_K + NH * NT32 * 2048;
constexpr unsigned WS_A = WS_V + NH * NT32 * 2048;
constexpr unsigned WS_W = WS_A + NT32 * KS_A * 1024;
constexpr unsigned WS_END = WS_W + 16 * KS_A * 2048;
static_assert(WS_FO + 256 * 8 <= WS_G, "flags");

DI unsigned cvtpk(float lo, float hi) { f32x2 v = {lo, hi}; bf16x2 b = __builtin_convertvector(v, bf16x2); return __builtin_bit_cast(unsigned, b); }
DI u32x4 pack8(const f32x16& a, int o, float s) {
  u32x4 w; w[0] = cvtpk(a[o + 0] * s, a[o + 1] * s); w[1] = cvtpk(a[o + 2] * s, a[o + 3] * s); w[2] = cvtpk(a[o + 4] * s, a[o + 5] * s); w[3] = cvtpk(a[o + 6] * s, a[o + 7] * s); return w; }
DI bf16x8 tr_frag(LAS const char* p0, LAS const char* p1) {
  s16x4 lo = __builtin_amdgcn_ds_read_tr16_b64_v4i16((LAS s16x4*)p0);
  s16x4 hi = __builtin_amdgcn_ds_read_tr16_b64_v4i16((LAS s16x4*)p1);
  return __builtin_bit_cast(bf16x8, __builtin_shufflevector(lo, hi, 0, 1, 2, 3, 4, 5, 6, 7)); }
DI void glds16(const char* sbase, unsigned voff, unsigned ldst) {
  asm volatile("s_mov_b32 m0, %0\n\ts_nop 0\n\tglobal_load_lds_dwordx4 %1, %2" ::"s"(ldst), "v"(voff), "s"(sbase) : "memory"); }
DI void glds16_sc1(const char* sbase, unsigned voff, unsigned ldst) {
  asm volatile("s_mov_b32 m0, %0\n\ts_nop 0\n\tglobal_load_lds_dwordx4 %1, %2 sc1" ::"s"(ldst), "v"(voff), "s"(sbase) : "memory"); }
DI void glds16c(int on, const char* sbase, unsigned voff, unsigned ldst) {
  asm volatile("s_cmp_lt_i32 %3, 1\n\ts_cbranch_scc1 1f\n\ts_mov_b32 m0, %0\n\ts_nop 0\n\tglobal_load_lds_dwordx4 %1, %2\n1:" ::"s"(ldst), "v"(voff), "s"(sbase), "s"(on) : "memory", "scc"); }
DI void l2_prefetch(const char* sbase, unsigned voff, unsigned ldst) {
  asm volatile("s_mov_b32 m0, %0\n\ts_nop 0\n\tglobal_load_lds_dword %1, %2 sc1" ::"s"(ldst), "v"(voff), "s"(sbase) : "memory"); }
#define STAMP_NEXT()
#define STAMP_DECL(kid)
#define STAMP_AT(i)
#define STAMP_END()
#define WAIT_VM(n) asm volatile("s_waitcnt vmcnt(" #n ")" ::: "memory")
#define WAITVM_BAR(n) asm volatile("s_waitcnt vmcnt(" #n ") lgkmcnt(0)\n\ts_barrier" ::: "memory")

typedef GAS u64 gu64;
DI void flag_set(char* ws, unsigned off, u64 epoch) { __hip_atomic_store((gu64*)(ws + off), epoch, RLX_AGENT); }
DI void flag_poll(u64 v, const gu64* p, u64 epoch) {
  unsigned n;
  asm volatile("s_mov_b32 %1, 0\n1:\n\tv_cmp_ne_u64 vcc, %2, %0\n\ts_cbranch_vccz 2f\n\ts_add_u32 %1, %1, 1\n\ts_cmp_gt_u32 %1, 0xffff\n\ts_cbranch_scc1 2f\n\ts_sleep 1\n\t"
               "global_load_dwordx2 %0, %3, off sc1\n\ts_waitcnt vmcnt(0)\n\ts_branch 1b\n2:"
               : "+v"(v), "=&s"(n) : "s"(epoch), "v"(p) : "vcc", "scc", "memory");
}
DI void flags_wait(const gu64* p, u64 epoch) { flag_poll(__hip_atomic_load(p, RLX_AGENT), p, epoch); }
DI void st16(__amdgpu_buffer_rsrc_t rs, unsigned voff, unsigned soff, u32x4 v) { __builtin_amdgcn_raw_buffer_store_b128(v, rs, voff + soff, 0, SC1); }

namespace lm {
constexpr int P1_X = 0, P1_WQ = 32768, P1_WG = 45056, P1_WU = 59520, P1_STAGE = 73984, P1_PF = 2 * P1_STAGE;
constexpr int AT_STAGE = 32768, AT_WT = 3 * AT_STAGE, AT_Q = 139264;
constexpr int P3_G = 131072, BYTES = 163840;
}

constexpr float QSCALE = 0.17677669529663687f * 1.4426950408889634f;
template <int WC> DI u64 phase1(LAS char* lds, const char* x, const char* wqkv, const char* wo, const char* wg, const char* wu, const char* wd, const char* gatew, char* ws, const char* out, int w, int j, int r, int wid, int lane) {
  using namespace lm;
  STAMP_DECL(0); STAMP_AT(0);
  const int wr = wid & 3, hi = lane >> 5, l31 = lane & 31;
  const unsigned ldsb = (unsigned)(uintptr_t)lds;
  const char* xsrc = x + (size_t)(r * 256 + wid * 32) * 2048;
  unsigned xoff[2];
#pragma unroll
  for (int e = 0; e < 2; ++e) xoff[e] = (unsigned)((lane >> 3) * 2048 + (((lane & 7) ^ ((4 * e + (lane >> 4)) & 7)) << 4));
  const char* wsrc[5]; unsigned woff[5], wdst[5], wstep[5];
#pragma unroll
  for (int m = 0; m < 5; ++m) {
    const int p = wid + 8 * m; const bool isq = p < 12, isg = p < 26;
    const int pp = isq ? p : (isg ? p - 12 : p - 26);
    wsrc[m] = isq ? wqkv : (isg ? wg : wu);
    wdst[m] = (unsigned)__builtin_amdgcn_readfirstlane((isq ? P1_WQ : (isg ? P1_WG : P1_WU)) + pp * 1024);
    wstep[m] = isq ? 64 * 6144 : 64 * 7168;
    const int cc = 64 * pp + lane, rq = cc / 12, pq = cc % 12, rg = cc / 14, pg = cc % 14;
    woff[m] = isq ? (unsigned)(rq * 6144 + ((pq >> 2) * 1024 + j * 32 + (pq & 3) * 8) * 2) : (unsigned)(rg * 7168 + (j * 112 + pg * 8) * 2);
  }
#define P1_DMA_X(on, q, t, st) glds16c(on, xsrc + (size_t)(t) * 128 + (q) * 8 * 2048, xoff[(q) & 1], ldsb + (st) + P1_X + (wid * 4 + (q)) * 1024)
#define P1_DMA_W(on, m, t, st) glds16c(on, wsrc[m] + (size_t)(t) * wstep[m], woff[m], ldsb + (st) + wdst[m])
  STAMP_AT(6);
  {
#pragma unroll
    for (int q = 0; q < 4; ++q) P1_DMA_X(1, q, 0, 0);
#pragma unroll
    for (int m = 0; m < 5; ++m) P1_DMA_W(1, m, 0, 0);
  }
  STAMP_AT(7);
  asm volatile("s_barrier" ::: "memory");
  STAMP_AT(8);
#pragma unroll
  for (int q = 0; q < 4; ++q) P1_DMA_X(1, q, 1, P1_STAGE);
#pragma unroll
  for (int m = 0; m < 5; ++m) P1_DMA_W(1, m, 1, P1_STAGE);
  unsigned pfoff, pfstep;
  {
    const int jj = j & 3, xg = j >> 2, row = (r * 4 + jj) * 2 + (wid & 1), l6 = lane % 6, l7 = (lane & 31) % 7;
    const unsigned ox = (unsigned)((r * 256 + jj * 64 + wid * 16 + (lane & 15)) * 2048);
    const unsigned oq = (unsigned)(row * 6144 + (l6 >> 1) * 2048 + xg * 256 + (l6 & 1) * 128), og = (unsigned)(row * 7168 + xg * 896 + l7 * 128);
    pfoff = wid < 4 ? ox : (wid < 6 ? oq : og); pfstep = wid < 4 ? 128 : (wid < 6 ? 64 * 6144 : 64 * 7168);
  }
  const char* pfl = (wid >= 6 ? (lane < 32 ? wg : wu) : (wid < 4 ? x : wqkv)) + pfoff;
  auto pf = [&](int t) { asm volatile("s_mov_b32 m0, %0\n\ts_nop 0\n\tglobal_load_lds_dword %1, off sc1" ::"s"(ldsb + P1_PF), "v"(pfl + (size_t)t * pfstep) : "memory"); };
  unsigned xa[4];
#pragma unroll
  for (int ks = 0; ks < 4; ++ks) xa[ks] = (unsigned)(P1_X + (wr * 64 + l31) * 128 + (((2 * ks + hi) ^ ((l31 >> 1) & 7)) << 4));
  const int q4 = (lane & 15) >> 2, p4 = lane & 3, h16 = (lane >> 4) & 1;
  const unsigned wqa = (unsigned)(P1_WQ + (8 * hi + q4) * 192 + 32 * h16 + 8 * p4);
  const unsigned wga = (unsigned)((h16 ? P1_WU : P1_WG) + (8 * hi + q4) * 224 + 8 * p4);
  f32x16 acc[2][5];
#pragma unroll
  for (int u = 0; u < 2; ++u)
#pragma unroll
    for (int b = 0; b < 5; ++b)
#pragma unroll
      for (int i = 0; i < 16; ++i) acc[u][b][i] = 0.f;
  bf16x8 xf[2][2], wf[2][5];
  auto ldx = [&](int set, int u, unsigned st, int ks) { xf[set][u] = *(LAS const bf16x8*)(lds + st + xa[ks] + u * 4096); };
  auto ldw = [&](int set, int b, unsigned st, int ks) {
    if (WC == 0 && b < 3) { LAS const char* p = lds + st + wqa + ks * 16 * 192 + b * 64; wf[set][b] = tr_frag(p, p + 4 * 192); }
    else { const int gb = WC == 0 ? b - 3 : b + 2; LAS const char* p = lds + st + wga + ks * 16 * 224 + gb * 32; wf[set][b] = tr_frag(p, p + 4 * 224); } };
  auto mma = [&](int set, int u, int b) {
    if (WC == 0 && b == 2) acc[u][b] = MFMA32(xf[set][u], wf[set][b], acc[u][b]); else acc[u][b] = MFMA32(wf[set][b], xf[set][u], acc[u][b]); };
#define P1_STEP(cs, nst, nks, DMA0, DMA1, DMA2, DMA3, DMA4) do { \
    ldx((cs) ^ 1, 0, nst, nks); ldx((cs) ^ 1, 1, nst, nks); mma(cs, 0, 0); DMA0; SBAR(); \
    ldw((cs) ^ 1, 0, nst, nks); mma(cs, 1, 0); SBAR(); mma(cs, 0, 1); DMA1; SBAR(); \
    ldw((cs) ^ 1, 1, nst, nks); mma(cs, 1, 1); SBAR(); mma(cs, 0, 2); DMA2; SBAR(); \
    ldw((cs) ^ 1, 2, nst, nks); mma(cs, 1, 2); SBAR(); mma(cs, 0, 3); DMA3; SBAR(); \
    ldw((cs) ^ 1, 3, nst, nks); mma(cs, 1, 3); SBAR(); ldw((cs) ^ 1, 4, nst, nks); mma(cs, 0, 4); DMA4; SBAR(); \
    mma(cs, 1, 4); SBAR(); } while (0)
#define NOP (void)0
  pf(2); pf(3);
  const u64 e0 = __hip_atomic_load((gu64*)(ws + WS_E), RLX_AGENT);
  l2_prefetch(gatew, (unsigned)(lane & 15) * 128, ldsb + P1_PF);
#define P1_W0 "13"
#define P1_W1 "5"
  STAMP_AT(9);
  asm volatile("s_waitcnt vmcnt(" P1_W0 ")\n\ts_barrier" ::: "memory");
  STAMP_AT(1);
  ldx(0, 0, 0, 0); ldx(0, 1, 0, 0);
#pragma unroll
  for (int b = 0; b < 5; ++b) ldw(0, b, 0, 0);
  unsigned cur = 0;
#pragma unroll 1
  for (int t = 0; t < 16; ++t) {
    const unsigned nxt = P1_STAGE - cur;
    const int m1 = t * (15 - t), m2 = 14 - t;
    const int tp = t + 4 < 16 ? t + 4 : 15;
    P1_STEP(0, cur, 1, P1_DMA_W(m1, 1, t + 1, nxt), P1_DMA_W(m1, 2, t + 1, nxt), P1_DMA_W(m1, 3, t + 1, nxt), P1_DMA_W(m1, 4, t + 1, nxt), pf(tp));
    P1_STEP(1, cur, 2, NOP, NOP, NOP, NOP, NOP);
    P1_STEP(0, cur, 3, NOP, NOP, NOP, NOP, NOP);
    asm volatile("s_cmp_lg_u32 %0, 0\n\ts_cbranch_scc1 1f\n\ts_waitcnt vmcnt(" P1_W1 ") lgkmcnt(0)\n\ts_branch 2f\n1:\n\ts_waitcnt vmcnt(1) lgkmcnt(0)\n2:\n\ts_barrier" ::"s"(t) : "memory", "scc");
    P1_STEP(1, nxt, 0, P1_DMA_X(m2, 0, t + 2, cur), P1_DMA_X(m2, 1, t + 2, cur), P1_DMA_X(m2, 2, t + 2, cur), P1_DMA_X(m2, 3, t + 2, cur), P1_DMA_W(m2, 0, t + 2, cur));
    cur = nxt;
  }
  WAITVM_BAR(0);
  STAMP_AT(2);
  const u64 epoch = (((u64)(unsigned)__builtin_amdgcn_readfirstlane((unsigned)(e0 >> 32)) << 32) | (unsigned)__builtin_amdgcn_readfirstlane((unsigned)e0)) + 1;
  const __amdgpu_buffer_rsrc_t rs = __builtin_amdgcn_make_buffer_rsrc(ws, 0, WS_END, 0x00020000);
  const unsigned l16 = lane * 16;
  const int gtok = r * 256 + j * 8 + wid;
  const u32x4* gxp = (const u32x4*)(x + (size_t)gtok * 2048) + lane * 2; const u32x4* ggp = (const u32x4*)gatew + lane * 2;
  const u32x4 ga0 = gxp[0], ga1 = gxp[1], gb0 = ggp[0], gb1 = ggp[1];
  SBAR();
  if (WC == 0) {
#pragma unroll
    for (int u = 0; u < 2; ++u) {
      const int tl = wr * 2 + u, T = r * 8 + tl;
      const u32x4 k0 = pack8(acc[u][1], 0, 1.f), k1 = pack8(acc[u][1], 8, 1.f), v0 = pack8(acc[u][2], 0, 1.f), v1 = pack8(acc[u][2], 8, 1.f);
      const unsigned go = (unsigned)(j * NT32 + T) * 2048;
      st16(rs, l16, WS_K + go, k0); st16(rs, l16, WS_K + go + 1024, k1); st16(rs, l16, WS_V + go, v0); st16(rs, l16, WS_V + go + 1024, v1);
      LAS char* kl = lds + tl * 2048 + l16;
      *(LAS u32x4*)(kl) = k0; *(LAS u32x4*)(kl + 1024) = k1; *(LAS u32x4*)(kl + 16384) = v0; *(LAS u32x4*)(kl + 16384 + 1024) = v1;
      LAS char* ql = lds + AT_Q + tl * 2048 + l16;
      *(LAS u32x4*)(ql) = pack8(acc[u][0], 0, QSCALE); *(LAS u32x4*)(ql + 1024) = pack8(acc[u][0], 8, QSCALE);
    }
  }
  SBAR();
  constexpr int NB = WC == 0 ? 2 : 5;
  u32x4 hw[2][NB];
#pragma unroll
  for (int u = 0; u < 2; ++u)
#pragma unroll
    for (int bb = 0; bb < NB; ++bb) {
      const int b = 5 - NB + bb;
      float h[8];
#pragma unroll
      for (int i = 0; i < 8; ++i) { const float g = acc[u][b][i], up = acc[u][b][8 + i];
        h[i] = g * __builtin_amdgcn_rcpf(1.f + __builtin_amdgcn_exp2f(-1.4426950408889634f * g)) * up; }
      hw[u][bb][0] = cvtpk(h[0], h[1]); hw[u][bb][1] = cvtpk(h[2], h[3]); hw[u][bb][2] = cvtpk(h[4], h[5]); hw[u][bb][3] = cvtpk(h[6], h[7]);
      PIN(hw[u][bb]);
    }
  {
    float s = 0.f;
#pragma unroll
    for (int e = 0; e < 4; ++e) { const unsigned ua0 = ga0[e], ub0 = gb0[e], ua1 = ga1[e], ub1 = gb1[e];
      s = __builtin_amdgcn_fdot2_f32_bf16(__builtin_bit_cast(bf16x2, ua0), __builtin_bit_cast(bf16x2, ub0), s, false);
      s = __builtin_amdgcn_fdot2_f32_bf16(__builtin_bit_cast(bf16x2, ua1), __builtin_bit_cast(bf16x2, ub1), s, false); }
#pragma unroll
    for (int o = 32; o >= 1; o >>= 1) s += __shfl_xor(s, o);
    const float g = __builtin_amdgcn_rcpf(1.f + __builtin_amdgcn_exp2f(-1.4426950408889634f * s));
    __hip_atomic_store((GAS unsigned*)(ws + WS_G + gtok * 4), __float_as_uint(g), RLX_AGENT);
  }
  SBAR();
  if (WC == 0) {
    WAIT_VM(1);
    if (lane == 0) flag_set(ws, WS_FKV + (unsigned)((j * 8 + r) * 4 + wr) * 8, epoch);
  }
  STAMP_AT(3);
  if (WC == 0 && wid == 0) {
    gu64* p = (gu64*)(ws + WS_FKV) + (j * 8 + ((r + 1 + ((lane >> 2) & 1)) & 7)) * 4 + (lane & 3);
    flags_wait(p, epoch);
  }
  asm volatile("s_waitcnt lgkmcnt(0)\n\ts_barrier" ::: "memory");
  STAMP_AT(4);
  {
    const char* kbase = ws + WS_K + (size_t)j * NT32 * 2048; const char* vbase = ws + WS_V + (size_t)j * NT32 * 2048;
#pragma unroll
    for (int t = 1; t < 3; ++t)
#pragma unroll
      for (int e = 0; e < 4; ++e)
        glds16_sc1((e < 2 ? kbase : vbase) + (size_t)((r + t) & 7) * 16384 + wid * 2048 + (e & 1) * 1024, l16, ldsb + t * AT_STAGE + (e >> 1) * 16384 + wid * 2048 + (e & 1) * 1024);
  }
  SBAR();
#pragma unroll
  for (int u = 0; u < 2; ++u)
#pragma unroll
    for (int bb = 0; bb < NB; ++bb) {
      const int T = r * 8 + wr * 2 + u, gb = WC == 0 ? bb : bb + 2;
      st16(rs, l16, WS_A + (unsigned)(T * KS_A + KS_O + j * 7 + gb) * 1024, hw[u][bb]);
    }
  SBAR();
  {
    const unsigned wl = (unsigned)((lane >> 2) * 2048 + (lane & 3) * 16);
#pragma unroll
    for (int m = 0; m < 5; ++m) { const int fl = wid + 8 * m < 36 ? wid + 8 * m : wid, f = 36 * w + fl, ks = f >> 5, fb = f & 31;
      const char* src = (ks < KS_O ? wo + (size_t)ks * 16 * 2048 : wd + (size_t)(ks - KS_O) * 16 * 2048) + fb * 64;
      glds16(src, wl, ldsb + AT_WT + (wid + 8 * m) * 1024); }
  }
  SBAR();
  STAMP_AT(5); STAMP_END();
  return epoch;
}

DI float halfmax(float m) { auto rr = __builtin_amdgcn_permlane32_swap(__float_as_uint(m), __float_as_uint(m), false, false); return fmaxf(__uint_as_float(rr[0]), __uint_as_float(rr[1])); }
DI void phase23(LAS char* lds, char* ws, char* out, u64 epoch, int w, int hd, int r, int xcd, int i, int wid, int lane) {
  using namespace lm;
  constexpr int NTILE = 8, STAGE = AT_STAGE;
  STAMP_DECL(1); STAMP_AT(0);
  const int T = r * 8 + wid;
  const char* kbase = ws + WS_K + (size_t)hd * NT32 * 2048; const char* vbase = ws + WS_V + (size_t)hd * NT32 * 2048;
  const unsigned ldsb = (unsigned)(uintptr_t)lds, l16 = lane * 16;
  const __amdgpu_buffer_rsrc_t rs = __builtin_amdgcn_make_buffer_rsrc(ws, 0, WS_END, 0x00020000);
#define GL2 glds16_sc1
#define P2_DMA1(e, t, st) GL2(((e) < 2 ? kbase : vbase) + (size_t)((r + (t)) & 7) * 16384 + wid * 2048 + ((e) & 1) * 1024, l16, ldsb + (st) + ((e) >> 1) * 16384 + wid * 2048 + ((e) & 1) * 1024)
  LAS const char* lp = lds + lane * 16;
  const bf16x8 q0 = *(LAS const bf16x8*)(lp + AT_Q + wid * 2048), q1 = *(LAS const bf16x8*)(lp + AT_Q + wid * 2048 + 1024);
  LAS const char* lp2 = lp + 2 * STAGE; asm volatile("" : "+v"(lp2));
#define LPS(st) ((st) >= 2 * STAGE ? lp2 - 2 * STAGE : lp)
#define KFR(st, b, h) (*(LAS const bf16x8*)(LPS(st) + (st) + (b) * 2048 + (h) * 1024))
#define VFR(st, b, h) (*(LAS const bf16x8*)(LPS(st) + (st) + 16384 + (b) * 2048 + (h) * 1024))
  f32x16 negm;
  {
    float m = -INFINITY;
#pragma unroll
    for (int b = 0; b < 4; ++b) {
      f32x16 s = {}; s = MFMA32(KFR(0, b, 0), q0, s); s = MFMA32(KFR(0, b, 1), q1, s);
#pragma unroll
      for (int e = 0; e < 16; ++e) m = fmaxf(m, s[e]);
    }
    m = halfmax(m);
#pragma unroll
    for (int e = 0; e < 16; ++e) negm[e] = -m;
    PIN(negm);
  }
  bf16x8 ones; { const bool on = ((lane & 1) == ((lane >> 4) & 1)); const __bf16 o = on ? (__bf16)1.0f : (__bf16)0.0f;
#pragma unroll
    for (int e = 0; e < 8; ++e) ones[e] = o; }
  PIN(ones);
  f32x16 o = {}; f32x4 ls0 = {}, ls1 = {};
  f32x16 sA, sB; u32x4 pA0, pA1, pB0 = {}, pB1 = {}; bf16x8 kA0, kA1, kB0, kB1, vA0, vA1, vB0 = {}, vB1 = {};
  sA = MFMA32(KFR(0, 0, 0), q0, negm); sA = MFMA32(KFR(0, 0, 1), q1, sA);
  kB0 = KFR(0, 1, 0); kB1 = KFR(0, 1, 1);
  STAMP_AT(1);
#define EX(S, i) S[i] = __builtin_amdgcn_exp2f(S[i])
#define PK(S, i) cvtpk(S[i], S[i + 1])
#define BF(x) __builtin_bit_cast(bf16x8, x)
#define P2_STEP(SX, SY, PX0, PX1, PY0, PY1, KX0, KX1, KY0, KY1, VX0, VX1, VY0, VY1, kst, kb, vst, vb, DMA) do { \
    SY = MFMA32(KY0, q0, negm); SBAR(); VX0 = VFR(vst, vb, 0); EX(SX, 0); EX(SX, 1); EX(SX, 2); EX(SX, 3); PIN(SX); SBAR(); \
    o = MFMA32(VY0, BF(PY0), o); SBAR(); VX1 = VFR(vst, vb, 1); EX(SX, 4); EX(SX, 5); EX(SX, 6); PX0[0] = PK(SX, 0); PX0[1] = PK(SX, 2); PIN(SX); PIN(PX0); SBAR(); \
    ls0 = MFMA16(ones, BF(PY0), ls0); SBAR(); EX(SX, 7); SBAR(); PX0[2] = PK(SX, 4); SBAR(); PX0[3] = PK(SX, 6); PIN(PX0); SBAR(); \
    SY = MFMA32(KY1, q1, SY); SBAR(); KX0 = KFR(kst, kb, 0); EX(SX, 8); EX(SX, 9); EX(SX, 10); EX(SX, 11); PIN(SX); SBAR(); \
    o = MFMA32(VY1, BF(PY1), o); SBAR(); KX1 = KFR(kst, kb, 1); EX(SX, 12); EX(SX, 13); EX(SX, 14); PX1[0] = PK(SX, 8); PX1[1] = PK(SX, 10); PIN(SX); PIN(PX1); SBAR(); \
    ls1 = MFMA16(ones, BF(PY1), ls1); SBAR(); EX(SX, 15); SBAR(); PX1[2] = PK(SX, 12); SBAR(); PX1[3] = PK(SX, 14); PIN(PX1); DMA; SBAR(); } while (0)
#define STEP_A(kst, kb, vst, vb, DMA) P2_STEP(sA, sB, pA0, pA1, pB0, pB1, kA0, kA1, kB0, kB1, vA0, vA1, vB0, vB1, kst, kb, vst, vb, DMA)
#define STEP_B(kst, kb, vst, vb, DMA) P2_STEP(sB, sA, pB0, pB1, pA0, pA1, kB0, kB1, kA0, kA1, vB0, vB1, vA0, vA1, kst, kb, vst, vb, DMA)
  constexpr int D = 4;
  const int tm = (xcd >> 1) * 4 + (i & 3), tn = (xcd & 1) * 8 + (i >> 2);
  const unsigned ab = WS_A + (unsigned)(tm * 4) * KS_A * 1024, wb = WS_W + (unsigned)tn * KS_A * 2048;
  bf16x8 a[D][4], wf[D][2];
  auto load = [&](int slot, int s) {
    const unsigned ks = (unsigned)((s < 28 ? KS_O + 8 * s : 8 * (s - 28)) + wid);
#pragma unroll
    for (int u = 0; u < 4; ++u) a[slot][u] = __builtin_bit_cast(bf16x8, __builtin_amdgcn_raw_buffer_load_b128(rs, l16, ab + (u * KS_A + ks) * 1024, SC1));
#pragma unroll
    for (int b = 0; b < 2; ++b) wf[slot][b] = __builtin_bit_cast(bf16x8, __builtin_amdgcn_raw_buffer_load_b128(rs, l16, wb + ks * 2048 + b * 1024, SC1));
  };
  gu64* const pkv = (gu64*)(ws + WS_FKV) + hd * 32 + (lane & 31);
  gu64* const pp1 = (gu64*)(ws + WS_FP1) + (lane < 32 ? r : (tm >> 1)) * 32 + (lane & 31);
  gu64* const pfw = (gu64*)(ws + WS_FW) + lane;
  u64 fkv = 0, f0 = 0, f1 = 0, f2 = 0, f3 = 0, f4 = 0, g2 = 0; unsigned gqu = 0;
#pragma unroll
  for (int t = 0; t < NTILE; ++t) {
    const unsigned st = (t % 3) * STAGE, sn = ((t + 1) % 3) * STAGE, sp = ((t + 2) % 3) * STAGE;
    STEP_A(st, 2, st, 0, if (t >= 2 && t + 1 < NTILE) P2_DMA1(1, t + 1, sn)); STEP_B(st, 3, st, 1, if (t >= 2 && t + 1 < NTILE) P2_DMA1(3, t + 1, sn));
    STEP_A(st, 4, st, 2, NOP); STEP_B(st, 5, st, 3, NOP); STEP_A(st, 6, st, 4, NOP); STEP_B(st, 7, st, 5, NOP);
    if (t == 0) { if (wid < 4) asm volatile("s_waitcnt vmcnt(13)\n\ts_barrier" ::: "memory"); else asm volatile("s_waitcnt vmcnt(19)\n\ts_barrier" ::: "memory");
      fkv = __hip_atomic_load(pkv, RLX_AGENT); SBAR(); }
    else if (t == 1) {
      WAIT_VM(0); flag_poll(fkv, pkv, epoch);
      asm volatile("s_barrier" ::: "memory"); }
    else {
      WAIT_VM(0);
      if (t == 6) { flag_poll(f0, pp1, epoch); flag_poll(f1, pfw, epoch); flag_poll(f2, pfw + 64, epoch); flag_poll(f3, pfw + 128, epoch); flag_poll(f4, pfw + 192, epoch); }
      asm volatile("s_barrier" ::: "memory");
    }
    if (t == 1 && threadIdx.x == 0) flag_set(ws, WS_FP1 + (unsigned)(r * 32 + hd) * 8, epoch);
    if (t == 2) {
      const int hi = lane >> 5, q4 = (lane & 15) >> 2, p4 = lane & 3, h16 = (lane >> 4) & 1;
      LAS const char* tp = lds + AT_WT + (4 * hi + q4) * 64 + 32 * h16 + 8 * p4;
#pragma unroll
      for (int m = 0; m < 5; ++m) if (wid + 8 * m < 36) { const int f = 36 * w + wid + 8 * m, ks = f >> 5, fb = f & 31;
        const bf16x8 v = tr_frag(tp + (wid + 8 * m) * 1024, tp + (wid + 8 * m) * 1024 + 8 * 64);
        st16(rs, l16, WS_W + (unsigned)(((fb >> 1) * KS_A + ks) * 2 + (fb & 1)) * 1024, __builtin_bit_cast(u32x4, v)); }
      SBAR();
    }
    if (t == 3 && threadIdx.x == 0) flag_set(ws, WS_FW + (unsigned)w * 8, epoch);
    if (t == 5) { f0 = __hip_atomic_load(pp1, RLX_AGENT); f1 = __hip_atomic_load(pfw, RLX_AGENT); f2 = __hip_atomic_load(pfw + 64, RLX_AGENT); f3 = __hip_atomic_load(pfw + 128, RLX_AGENT); f4 = __hip_atomic_load(pfw + 192, RLX_AGENT); SBAR(); }
    if (t == 6) {
      gqu = __hip_atomic_load((GAS unsigned*)(ws + WS_G + (T * 32 + (lane & 31)) * 4), RLX_AGENT);
      g2 = __hip_atomic_load((gu64*)(ws + WS_G + (tm * 128 + lane * 2) * 4), RLX_AGENT);
      SBAR();
    }
    if (t == 7) {
#pragma unroll
      for (int s = 0; s < D; ++s) load(s, s);
      SBAR();
    }
    STEP_A(sn, 0, st, 6, if (t >= 1 && t + 2 < NTILE) P2_DMA1(0, t + 2, sp)); STEP_B(sn, 1, st, 7, if (t >= 1 && t + 2 < NTILE) P2_DMA1(2, t + 2, sp));
  }
  o = MFMA32(vB0, BF(pB0), o); o = MFMA32(vB1, BF(pB1), o);
  ls0 = MFMA16(ones, BF(pB0), ls0); ls1 = MFMA16(ones, BF(pB1), ls1);
  STAMP_AT(2);
  const float gq = __uint_as_float(gqu);

  float l = (lane & 16) ? ls0[1] + ls1[1] : ls0[0] + ls1[0];
  float amax = 0.f;
#pragma unroll
  for (int e = 0; e < 16; ++e) amax = fmaxf(amax, fabsf(o[e]));
  const bool bad = !(l < 1.0995116e12f) || !(amax < INFINITY);
  if (__builtin_expect(__any(bad), 0)) {
    float m = -INFINITY; l = 0.f;
#pragma unroll
    for (int e = 0; e < 16; ++e) o[e] = 0.f;
    for (int b = 0; b < NT32; ++b) {
      const unsigned ko = WS_K + (unsigned)(hd * NT32 + b) * 2048, vo = WS_V + (unsigned)(hd * NT32 + b) * 2048;
      const bf16x8 k0 = BF(__builtin_amdgcn_raw_buffer_load_b128(rs, l16, ko, SC1)), k1 = BF(__builtin_amdgcn_raw_buffer_load_b128(rs, l16, ko + 1024, SC1));
      const bf16x8 v0 = BF(__builtin_amdgcn_raw_buffer_load_b128(rs, l16, vo, SC1)), v1 = BF(__builtin_amdgcn_raw_buffer_load_b128(rs, l16, vo + 1024, SC1));
      f32x16 s = {}; s = MFMA32(k0, q0, s); s = MFMA32(k1, q1, s);
      float mb = s[0];
#pragma unroll
      for (int e = 1; e < 16; ++e) mb = fmaxf(mb, s[e]);
      const float mn = fmaxf(m, halfmax(mb)); const float al = __builtin_amdgcn_exp2f(m - mn); m = mn;
#pragma unroll
      for (int e = 0; e < 16; ++e) { s[e] = __builtin_amdgcn_exp2f(s[e] - mn); o[e] *= al; }
      const u32x4 w0 = pack8(s, 0, 1.f), w1 = pack8(s, 8, 1.f);
      float rsum = 0.f;
#pragma unroll
      for (int e = 0; e < 4; ++e) rsum += __uint_as_float(w0[e] << 16) + __uint_as_float(w0[e] & 0xffff0000u) + __uint_as_float(w1[e] << 16) + __uint_as_float(w1[e] & 0xffff0000u);
      { auto rr = __builtin_amdgcn_permlane32_swap(__float_as_uint(rsum), __float_as_uint(rsum), false, false); rsum = __uint_as_float(rr[0]) + __uint_as_float(rr[1]); }
      l = l * al + rsum;
      o = MFMA32(v0, BF(w0), o); o = MFMA32(v1, BF(w1), o);
    }
  }
  const float sc = gq / l;
  const unsigned oo = WS_A + (unsigned)(T * KS_A + hd * 2) * 1024;
  st16(rs, l16, oo, pack8(o, 0, sc)); st16(rs, l16, oo + 1024, pack8(o, 8, sc));
  LAS float* gl = (LAS float*)(lds + P3_G + wid * 512);
  *(LAS f32x2*)(gl + lane * 2) = (f32x2){1.f - __uint_as_float((unsigned)g2), 1.f - __uint_as_float((unsigned)(g2 >> 32))};
  WAITVM_BAR(0);
  if (threadIdx.x == 0) flag_set(ws, WS_FO + (unsigned)(r * 32 + hd) * 8, epoch);
  STAMP_AT(3); STAMP_END();

  STAMP_NEXT(); STAMP_AT(0);
  const int l31 = lane & 31;
  f32x16 acc[4][2];
#pragma unroll
  for (int u = 0; u < 4; ++u)
#pragma unroll
    for (int b = 0; b < 2; ++b)
#pragma unroll
      for (int e = 0; e < 16; ++e) acc[u][b][e] = 0.f;
  auto mma = [&](int d) {
#pragma unroll
    for (int u = 0; u < 4; ++u)
#pragma unroll
      for (int b = 0; b < 2; ++b) acc[u][b] = MFMA32(wf[d][b], a[d][u], acc[u][b]); };
#pragma unroll 1
  for (int so = 0; so < 28 - D; so += D) {
#pragma unroll
    for (int d = 0; d < D; ++d) { mma(d); SBAR(); load(d, so + d + D); SBAR(); }
  }
  flags_wait((gu64*)(ws + WS_FO) + (tm >> 1) * 32 + (lane & 31), epoch);
#pragma unroll
  for (int d = 0; d < D; ++d) { mma(d); SBAR(); load(d, 28 + d); SBAR(); }
#pragma unroll
  for (int u = 0; u < 4; ++u) { const float gt = gl[u * 32 + l31];
#pragma unroll
    for (int b = 0; b < 2; ++b)
#pragma unroll
      for (int e = 0; e < 16; ++e) acc[u][b][e] *= gt;
    SBAR(); }
#pragma unroll
  for (int d = 0; d < D; ++d) { mma(d); SBAR(); load(d, 32 + d); SBAR(); }
#pragma unroll
  for (int d = 0; d < D; ++d) mma(d);
  STAMP_AT(1);
  int tz = threadIdx.x; asm volatile("" : "+v"(tz));
  const int ln = tz & 63;
  LAS f32x4* red = (LAS f32x4*)lds + ln;
  f32x4 res[4];
#pragma unroll
  for (int ph = 0; ph < 2; ++ph) {
    if (ph) __syncthreads();
#pragma unroll
    for (int u = 0; u < 4; ++u)
#pragma unroll
      for (int b = 0; b < 2; ++b)
#pragma unroll
        for (int e = 0; e < 2; ++e) { const int oi = 8 * ph + 4 * e;
          red[((wid * 8 + u * 2 + b) * 2 + e) * 64] = (f32x4){acc[u][b][oi], acc[u][b][oi + 1], acc[u][b][oi + 2], acc[u][b][oi + 3]}; }
    __syncthreads();
    if (ph == 0) STAMP_AT(2);
#pragma unroll
    for (int e = 0; e < 2; ++e) { f32x4 s = red[((0 * 8 + wid) * 2 + e) * 64];
#pragma unroll
      for (int g = 1; g < 8; ++g) s += red[((g * 8 + wid) * 2 + e) * 64];
      res[2 * ph + e] = s; }
  }
  {
    const int tok = tm * 128 + (wid >> 1) * 32 + (ln & 31), f0 = tn * 64 + (wid & 1) * 32 + 4 * (ln >> 5);
    char* op = out + (size_t)tok * 2048 + f0 * 2;
#pragma unroll
    for (int e = 0; e < 4; ++e) { u32x2 v; v[0] = cvtpk(res[e][0], res[e][1]); v[1] = cvtpk(res[e][2], res[e][3]); *(u32x2*)(op + e * 16) = v; }
  }
  STAMP_AT(3); STAMP_END();
}

__global__ __launch_bounds__(512, 2) void mega(const char* x, const char* wqkv, const char* wo, const char* wg, const char* wu, const char* wd, const char* gatew, char* ws, char* out) {
  extern __shared__ __attribute__((aligned(16))) char smem[];
  LAS char* lds = (LAS char*)smem;
  const int tid = threadIdx.x, lane = tid & 63, wid = __builtin_amdgcn_readfirstlane(tid >> 6);
  const int w = blockIdx.x, xcd = w & 7, i = w >> 3, j = xcd * 4 + (i & 3), r = i >> 2;
  u64 epoch;
  if (wid < 4) epoch = phase1<0>(lds, x, wqkv, wo, wg, wu, wd, gatew, ws, out, w, j, r, wid, lane); else epoch = phase1<1>(lds, x, wqkv, wo, wg, wu, wd, gatew, ws, out, w, j, r, wid, lane);
  phase23(lds, ws, out, epoch, w, j, r, xcd, i, wid, lane);
  if (w == 0 && tid == 0) flag_set(ws, WS_E, epoch);
}

extern "C" void kernel_launch(void* const* d_in, const int* in_sizes, int n_in, void* d_out, int out_size, void* d_ws, size_t ws_size, hipStream_t stream) {
  (void)hipFuncSetAttribute((const void*)mega, hipFuncAttributeMaxDynamicSharedMemorySize, lm::BYTES);
  mega<<<256, 512, lm::BYTES, stream>>>((const char*)d_in[0], (const char*)d_in[1], (const char*)d_in[2], (const char*)d_in[4], (const char*)d_in[5], (const char*)d_in[6],
                                        (const char*)d_in[7], (char*)d_ws, (char*)d_out);
}
```

```cpp
#include <hip/hip_runtime.h>
#include <stdint.h>

typedef __bf16 bf16x8 __attribute__((ext_vector_type(8)));
typedef __bf16 bf16x2 __attribute__((ext_vector_type(2)));
typedef short s16x4 __attribute__((ext_vector_type(4)));
typedef float f32x16 __attribute__((ext_vector_type(16)));
typedef float f32x4 __attribute__((ext_vector_type(4)));
typedef float f32x2 __attribute__((ext_vector_type(2)));
typedef unsigned u32x4 __attribute__((ext_vector_type(4)));
typedef unsigned u32x2 __attribute__((ext_vector_type(2)));
typedef unsigned long long u64;
#define LAS __attribute__((address_space(3)))
#define GAS __attribute__((address_space(1)))
#define DI __device__ __forceinline__
#define SBAR() __builtin_amdgcn_sched_barrier(0)
#define PIN(x) asm volatile("" : "+v"(x))
#define MFMA32(a, b, c) __builtin_amdgcn_mfma_f32_32x32x16_bf16((a), (b), (c), 0, 0, 0)
#define MFMA16(a, b, c) __builtin_amdgcn_mfma_f32_16x16x32_bf16((a), (b), (c), 0, 0, 0)
#define RLX_AGENT __ATOMIC_RELAXED, __HIP_MEMORY_SCOPE_AGENT
#define SC1 16
#define OUT_AUX 0

constexpr int SEQ = 2048, DM = 1024, FF = 3584, NH = 32;
constexpr int NT32 = SEQ / 32;
constexpr int KS_O = DM / 16;
constexpr int KS_H = FF / 16;
constexpr int KS_A = KS_O + KS_H;
constexpr unsigned WS_E = 0;
constexpr unsigned WS_FKV = 256;
constexpr unsigned WS_FP1 = WS_FKV + 32 * 32 * 8;
constexpr unsigned WS_FW = WS_FP1 + 256 * 8;
constexpr unsigned WS_FO = WS_FW + 256 * 8;
constexpr unsigned WS_G = 16384;
constexpr unsigned WS_K = 32768;
constexpr unsigned WS_V = WS_K + NH * NT32 * 2048;
constexpr unsigned WS_A = WS_V + NH * NT32 * 2048;
constexpr unsigned WS_W = WS_A + NT32 * KS_A * 1024;
constexpr unsigned WS_END = WS_W + 16 * KS_A * 2048;
static_assert(WS_FO + 256 * 8 <= WS_G, "flags");

DI unsigned cvtpk(float lo, float hi) { f32x2 v = {lo, hi}; bf16x2 b = __builtin_convertvector(v, bf16x2); return __builtin_bit_cast(unsigned, b); }
DI u32x4 pack8(const f32x16& a, int o, float s) {
  u32x4 w; w[0] = cvtpk(a[o + 0] * s, a[o + 1] * s); w[1] = cvtpk(a[o + 2] * s, a[o + 3] * s); w[2] = cvtpk(a[o + 4] * s, a[o + 5] * s); w[3] = cvtpk(a[o + 6] * s, a[o + 7] * s); return w; }
DI bf16x8 tr_frag(LAS const char* p0, LAS const char* p1) {
  s16x4 lo = __builtin_amdgcn_ds_read_tr16_b64_v4i16((LAS s16x4*)p0);
  s16x4 hi = __builtin_amdgcn_ds_read_tr16_b64_v4i16((LAS s16x4*)p1);
  return __builtin_bit_cast(bf16x8, __builtin_shufflevector(lo, hi, 0, 1, 2, 3, 4, 5, 6, 7)); }
DI void glds16(const char* sbase, unsigned voff, unsigned ldst) {
  asm volatile("s_mov_b32 m0, %0\n\ts_nop 0\n\tglobal_load_lds_dwordx4 %1, %2" ::"s"(ldst), "v"(voff), "s"(sbase) : "memory"); }
DI void glds16_sc1(const char* sbase, unsigned voff, unsigned ldst) {
  asm volatile("s_mov_b32 m0, %0\n\ts_nop 0\n\tglobal_load_lds_dwordx4 %1, %2 sc1" ::"s"(ldst), "v"(voff), "s"(sbase) : "memory"); }
DI void glds16c(int on, const char* sbase, unsigned voff, unsigned ldst) {
  asm volatile("s_cmp_lt_i32 %3, 1\n\ts_cbranch_scc1 1f\n\ts_mov_b32 m0, %0\n\ts_nop 0\n\tglobal_load_lds_dwordx4 %1, %2\n1:" ::"s"(ldst), "v"(voff), "s"(sbase), "s"(on) : "memory", "scc"); }
DI void l2_prefetch(const char* sbase, unsigned voff, unsigned ldst) {
  asm volatile("s_mov_b32 m0, %0\n\ts_nop 0\n\tglobal_load_lds_dword %1, %2 sc1" ::"s"(ldst), "v"(voff), "s"(sbase) : "memory"); }
#define STAMP_NEXT()
#define STAMP_DECL(kid)
#define STAMP_AT(i)
#define STAMP_END()
#define WAIT_VM(n) asm volatile("s_waitcnt vmcnt(" #n ")" ::: "memory")
#define WAITVM_BAR(n) asm volatile("s_waitcnt vmcnt(" #n ") lgkmcnt(0)\n\ts_barrier" ::: "memory")

typedef GAS u64 gu64;
DI void flag_set(char* ws, unsigned off, u64 epoch) { __hip_atomic_store((gu64*)(ws + off), epoch, RLX_AGENT); }
DI void flag_poll(u64 v, const gu64* p, u64 epoch) {
  unsigned n;
  asm volatile("s_mov_b32 %1, 0\n1:\n\tv_cmp_ne_u64 vcc, %2, %0\n\ts_cbranch_vccz 2f\n\ts_add_u32 %1, %1, 1\n\ts_cmp_gt_u32 %1, 0xffff\n\ts_cbranch_scc1 2f\n\ts_sleep 1\n\t"
               "global_load_dwordx2 %0, %3, off sc1\n\ts_waitcnt vmcnt(0)\n\ts_branch 1b\n2:"
               : "+v"(v), "=&s"(n) : "s"(epoch), "v"(p) : "vcc", "scc", "memory");
}
DI void flags_wait(const gu64* p, u64 epoch) { flag_poll(__hip_atomic_load(p, RLX_AGENT), p, epoch); }
DI void st16(__amdgpu_buffer_rsrc_t rs, unsigned voff, unsigned soff, u32x4 v) { __builtin_amdgcn_raw_buffer_store_b128(v, rs, voff + soff, 0, SC1); }

namespace lm {
constexpr int P1_X = 0, P1_WQ = 32768, P1_WG = 45056, P1_WU = 59520, P1_STAGE = 73984, P1_PF = 2 * P1_STAGE;
constexpr int AT_STAGE = 32768, AT_WT = 3 * AT_STAGE, AT_Q = 139264;
constexpr int P3_G = 131072, BYTES = 163840;
}

constexpr float QSCALE = 0.17677669529663687f * 1.4426950408889634f;
template <int WC> DI u64 phase1(LAS char* lds, const char* x, const char* wqkv, const char* wo, const char* wg, const char* wu, const char* wd, const char* gatew, char* ws, const char* out, int w, int j, int r, int wid, int lane) {
  using namespace lm;
  STAMP_DECL(0); STAMP_AT(0);
  const int wr = wid & 3, hi = lane >> 5, l31 = lane & 31;
  const unsigned ldsb = (unsigned)(uintptr_t)lds;
  const char* xsrc = x + (size_t)(r * 256 + wid * 32) * 2048;
  unsigned xoff[2];
#pragma unroll
  for (int e = 0; e < 2; ++e) xoff[e] = (unsigned)((lane >> 3) * 2048 + (((lane & 7) ^ ((4 * e + (lane >> 4)) & 7)) << 4));
  const char* wsrc[5]; unsigned woff[5], wdst[5], wstep[5];
#pragma unroll
  for (int m = 0; m < 5; ++m) {
    const int p = wid + 8 * m; const bool isq = p < 12, isg = p < 26;
    const int pp = isq ? p : (isg ? p - 12 : p - 26);
    wsrc[m] = isq ? wqkv : (isg ? wg : wu);
    wdst[m] = (unsigned)__builtin_amdgcn_readfirstlane((isq ? P1_WQ : (isg ? P1_WG : P1_WU)) + pp * 1024);
    wstep[m] = isq ? 64 * 6144 : 64 * 7168;
    const int cc = 64 * pp + lane, rq = cc / 12, pq = cc % 12, rg = cc / 14, pg = cc % 14;
    woff[m] = isq ? (unsigned)(rq * 6144 + ((pq >> 2) * 1024 + j * 32 + (pq & 3) * 8) * 2) : (unsigned)(rg * 7168 + (j * 112 + pg * 8) * 2);
  }
#define P1_DMA_X(on, q, t, st) glds16c(on, xsrc + (size_t)(t) * 128 + (q) * 8 * 2048, xoff[(q) & 1], ldsb + (st) + P1_X + (wid * 4 + (q)) * 1024)
#define P1_DMA_W(on, m, t, st) glds16c(on, wsrc[m] + (size_t)(t) * wstep[m], woff[m], ldsb + (st) + wdst[m])
  STAMP_AT(6);
  {
#pragma unroll
    for (int q = 0; q < 4; ++q) P1_DMA_X(1, q, 0, 0);
#pragma unroll
    for (int m = 0; m < 5; ++m) P1_DMA_W(1, m, 0, 0);
  }
  STAMP_AT(7);
  asm volatile("s_barrier" ::: "memory");
  STAMP_AT(8);
#pragma unroll
  for (int q = 0; q < 4; ++q) P1_DMA_X(1, q, 1, P1_STAGE);
#pragma unroll
  for (int m = 0; m < 5; ++m) P1_DMA_W(1, m, 1, P1_STAGE);
  unsigned pfoff, pfstep;
  {
    const int jj = j & 3, xg = j >> 2, row = (r * 4 + jj) * 2 + (wid & 1), l6 = lane % 6, l7 = (lane & 31) % 7;
    const unsigned ox = (unsigned)((r * 256 + jj * 64 + wid * 16 + (lane & 15)) * 2048);
    const unsigned oq = (unsigned)(row * 6144 + (l6 >> 1) * 2048 + xg * 256 + (l6 & 1) * 128), og = (unsigned)(row * 7168 + xg * 896 + l7 * 128);
    pfoff = wid < 4 ? ox : (wid < 6 ? oq : og); pfstep = wid < 4 ? 128 : (wid < 6 ? 64 * 6144 : 64 * 7168);
  }
  const char* pfl = (wid >= 6 ? (lane < 32 ? wg : wu) : (wid < 4 ? x : wqkv)) + pfoff;
  auto pf = [&](int t) { asm volatile("s_mov_b32 m0, %0\n\ts_nop 0\n\tglobal_load_lds_dword %1, off sc1" ::"s"(ldsb + P1_PF), "v"(pfl + (size_t)t * pfstep) : "memory"); };
  unsigned xa[4];
#pragma unroll
  for (int ks = 0; ks < 4; ++ks) xa[ks] = (unsigned)(P1_X + (wr * 64 + l31) * 128 + (((2 * ks + hi) ^ ((l31 >> 1) & 7)) << 4));
  const int q4 = (lane & 15) >> 2, p4 = lane & 3, h16 = (lane >> 4) & 1;
  const unsigned wqa = (unsigned)(P1_WQ + (8 * hi + q4) * 192 + 32 * h16 + 8 * p4);
  const unsigned wga = (unsigned)((h16 ? P1_WU : P1_WG) + (8 * hi + q4) * 224 + 8 * p4);
  f32x16 acc[2][5];
#pragma unroll
  for (int u = 0; u < 2; ++u)
#pragma unroll
    for (int b = 0; b < 5; ++b)
#pragma unroll
      for (int i = 0; i < 16; ++i) acc[u][b][i] = 0.f;
  bf16x8 xf[2][2], wf[2][5];
  auto ldx = [&](int set, int u, unsigned st, int ks) { xf[set][u] = *(LAS const bf16x8*)(lds + st + xa[ks] + u * 4096); };
  auto ldw = [&](int set, int b, unsigned st, int ks) {
    if (WC == 0 && b < 3) { LAS const char* p = lds + st + wqa + ks * 16 * 192 + b * 64; wf[set][b] = tr_frag(p, p + 4 * 192); }
    else { const int gb = WC == 0 ? b - 3 : b + 2; LAS const char* p = lds + st + wga + ks * 16 * 224 + gb * 32; wf[set][b] = tr_frag(p, p + 4 * 224); } };
  auto mma = [&](int set, int u, int b) {
    if (WC == 0 && b == 2) acc[u][b] = MFMA32(xf[set][u], wf[set][b], acc[u][b]); else acc[u][b] = MFMA32(wf[set][b], xf[set][u], acc[u][b]); };
#define P1_STEP(cs, nst, nks, DMA0, DMA1, DMA2, DMA3, DMA4) do { \
    ldx((cs) ^ 1, 0, nst, nks); ldx((cs) ^ 1, 1, nst, nks); mma(cs, 0, 0); DMA0; SBAR(); \
    ldw((cs) ^ 1, 0, nst, nks); mma(cs, 1, 0); SBAR(); mma(cs, 0, 1); DMA1; SBAR(); \
    ldw((cs) ^ 1, 1, nst, nks); mma(cs, 1, 1); SBAR(); mma(cs, 0, 2); DMA2; SBAR(); \
    ldw((cs) ^ 1, 2, nst, nks); mma(cs, 1, 2); SBAR(); mma(cs, 0, 3); DMA3; SBAR(); \
    ldw((cs) ^ 1, 3, nst, nks); mma(cs, 1, 3); SBAR(); ldw((cs) ^ 1, 4, nst, nks); mma(cs, 0, 4); DMA4; SBAR(); \
    mma(cs, 1, 4); SBAR(); } while (0)
#define NOP (void)0
  pf(2); pf(3);
  const u64 e0 = __hip_atomic_load((gu64*)(ws + WS_E), RLX_AGENT);
  l2_prefetch(gatew, (unsigned)(lane & 15) * 128, ldsb + P1_PF);
#define P1_W0 "13"
#define P1_W1 "5"
  STAMP_AT(9);
  asm volatile("s_waitcnt vmcnt(" P1_W0 ")\n\ts_barrier" ::: "memory");
  STAMP_AT(1);
  ldx(0, 0, 0, 0); ldx(0, 1, 0, 0);
#pragma unroll
  for (int b = 0; b < 5; ++b) ldw(0, b, 0, 0);
  unsigned cur = 0;
#pragma unroll 1
  for (int t = 0; t < 16; ++t) {
    const unsigned nxt = P1_STAGE - cur;
    const int m1 = t * (15 - t), m2 = 14 - t;
    const int tp = t + 4 < 16 ? t + 4 : 15;
    P1_STEP(0, cur, 1, P1_DMA_W(m1, 1, t + 1, nxt), P1_DMA_W(m1, 2, t + 1, nxt), P1_DMA_W(m1, 3, t + 1, nxt), P1_DMA_W(m1, 4, t + 1, nxt), pf(tp));
    P1_STEP(1, cur, 2, NOP, NOP, NOP, NOP, NOP);
    P1_STEP(0, cur, 3, NOP, NOP, NOP, NOP, NOP);
    asm volatile("s_cmp_lg_u32 %0, 0\n\ts_cbranch_scc1 1f\n\ts_waitcnt vmcnt(" P1_W1 ") lgkmcnt(0)\n\ts_branch 2f\n1:\n\ts_waitcnt vmcnt(1) lgkmcnt(0)\n2:\n\ts_barrier" ::"s"(t) : "memory", "scc");
    P1_STEP(1, nxt, 0, P1_DMA_X(m2, 0, t + 2, cur), P1_DMA_X(m2, 1, t + 2, cur), P1_DMA_X(m2, 2, t + 2, cur), P1_DMA_X(m2, 3, t + 2, cur), P1_DMA_W(m2, 0, t + 2, cur));
    cur = nxt;
  }
  WAITVM_BAR(0);
  STAMP_AT(2);
  const u64 epoch = (((u64)(unsigned)__builtin_amdgcn_readfirstlane((unsigned)(e0 >> 32)) << 32) | (unsigned)__builtin_amdgcn_readfirstlane((unsigned)e0)) + 1;
  const __amdgpu_buffer_rsrc_t rs = __builtin_amdgcn_make_buffer_rsrc(ws, 0, WS_END, 0x00020000);
  const unsigned l16 = lane * 16;
  const int gtok = r * 256 + j * 8 + wid;
  const u32x4* gxp = (const u32x4*)(x + (size_t)gtok * 2048) + lane * 2; const u32x4* ggp = (const u32x4*)gatew + lane * 2;
  const u32x4 ga0 = gxp[0], ga1 = gxp[1], gb0 = ggp[0], gb1 = ggp[1];
  SBAR();
  if (WC == 0) {
#pragma unroll
    for (int u = 0; u < 2; ++u) {
      const int tl = wr * 2 + u, T = r * 8 + tl;
      const u32x4 k0 = pack8(acc[u][1], 0, 1.f), k1 = pack8(acc[u][1], 8, 1.f), v0 = pack8(acc[u][2], 0, 1.f), v1 = pack8(acc[u][2], 8, 1.f);
      const unsigned go = (unsigned)(j * NT32 + T) * 2048;
      st16(rs, l16, WS_K + go, k0); st16(rs, l16, WS_K + go + 1024, k1); st16(rs, l16, WS_V + go, v0); st16(rs, l16, WS_V + go + 1024, v1);
      LAS char* kl = lds + tl * 2048 + l16;
      *(LAS u32x4*)(kl) = k0; *(LAS u32x4*)(kl + 1024) = k1; *(LAS u32x4*)(kl + 16384) = v0; *(LAS u32x4*)(kl + 16384 + 1024) = v1;
      LAS char* ql = lds + AT_Q + tl * 2048 + l16;
      *(LAS u32x4*)(ql) = pack8(acc[u][0], 0, QSCALE); *(LAS u32x4*)(ql + 1024) = pack8(acc[u][0], 8, QSCALE);
    }
  }
  SBAR();
  constexpr int NB = WC == 0 ? 2 : 5;
  u32x4 hw[2][NB];
#pragma unroll
  for (int u = 0; u < 2; ++u)
#pragma unroll
    for (int bb = 0; bb < NB; ++bb) {
      const int b = 5 - NB + bb;
      float h[8];
#pragma unroll
      for (int i = 0; i < 8; ++i) { const float g = acc[u][b][i], up = acc[u][b][8 + i];
        h[i] = g * __builtin_amdgcn_rcpf(1.f + __builtin_amdgcn_exp2f(-1.4426950408889634f * g)) * up; }
      hw[u][bb][0] = cvtpk(h[0], h[1]); hw[u][bb][1] = cvtpk(h[2], h[3]); hw[u][bb][2] = cvtpk(h[4], h[5]); hw[u][bb][3] = cvtpk(h[6], h[7]);
      PIN(hw[u][bb]);
    }
  {
    float s = 0.f;
#pragma unroll
    for (int e = 0; e < 4; ++e) { const unsigned ua0 = ga0[e], ub0 = gb0[e], ua1 = ga1[e], ub1 = gb1[e];
      s = __builtin_amdgcn_fdot2_f32_bf16(__builtin_bit_cast(bf16x2, ua0), __builtin_bit_cast(bf16x2, ub0), s, false);
      s = __builtin_amdgcn_fdot2_f32_bf16(__builtin_bit_cast(bf16x2, ua1), __builtin_bit_cast(bf16x2, ub1), s, false); }
#pragma unroll
    for (int o = 32; o >= 1; o >>= 1) s += __shfl_xor(s, o);
    const float g = __builtin_amdgcn_rcpf(1.f + __builtin_amdgcn_exp2f(-1.4426950408889634f * s));
    __hip_atomic_store((GAS unsigned*)(ws + WS_G + gtok * 4), __float_as_uint(g), RLX_AGENT);
  }
  SBAR();
  if (WC == 0) {
    WAIT_VM(1);
    if (lane == 0) flag_set(ws, WS_FKV + (unsigned)((j * 8 + r) * 4 + wr) * 8, epoch);
  }
  STAMP_AT(3);
  if (WC == 0 && wid == 0) {
    gu64* p = (gu64*)(ws + WS_FKV) + (j * 8 + ((r + 1 + ((lane >> 2) & 1)) & 7)) * 4 + (lane & 3);
    flags_wait(p, epoch);
  }
  asm volatile("s_waitcnt lgkmcnt(0)\n\ts_barrier" ::: "memory");
  STAMP_AT(4);
  {
    const char* kbase = ws + WS_K + (size_t)j * NT32 * 2048; const char* vbase = ws + WS_V + (size_t)j * NT32 * 2048;
#pragma unroll
    for (int t = 1; t < 3; ++t)
#pragma unroll
      for (int e = 0; e < 4; ++e)
        glds16_sc1((e < 2 ? kbase : vbase) + (size_t)((r + t) & 7) * 16384 + wid * 2048 + (e & 1) * 1024, l16, ldsb + t * AT_STAGE + (e >> 1) * 16384 + wid * 2048 + (e & 1) * 1024);
  }
  SBAR();
#pragma unroll
  for (int u = 0; u < 2; ++u)
#pragma unroll
    for (int bb = 0; bb < NB; ++bb) {
      const int T = r * 8 + wr * 2 + u, gb = WC == 0 ? bb : bb + 2;
      st16(rs, l16, WS_A + (unsigned)(T * KS_A + KS_O + j * 7 + gb) * 1024, hw[u][bb]);
    }
  SBAR();
  {
    const unsigned wl = (unsigned)((lane >> 2) * 2048 + (lane & 3) * 16);
#pragma unroll
    for (int m = 0; m < 5; ++m) { const int fl = wid + 8 * m < 36 ? wid + 8 * m : wid, f = 36 * w + fl, ks = f >> 5, fb = f & 31;
      const char* src = (ks < KS_O ? wo + (size_t)ks * 16 * 2048 : wd + (size_t)(ks - KS_O) * 16 * 2048) + fb * 64;
      glds16(src, wl, ldsb + AT_WT + (wid + 8 * m) * 1024); }
  }
  SBAR();
  STAMP_AT(5); STAMP_END();
  return epoch;
}

DI float halfmax(float m) { auto rr = __builtin_amdgcn_permlane32_swap(__float_as_uint(m), __float_as_uint(m), false, false); return fmaxf(__uint_as_float(rr[0]), __uint_as_float(rr[1])); }
DI void phase23(LAS char* lds, char* ws, char* out, u64 epoch, int w, int hd, int r, int xcd, int i, int wid, int lane) {
  using namespace lm;
  constexpr int NTILE = 8, STAGE = AT_STAGE;
  STAMP_DECL(1); STAMP_AT(0);
  const int T = r * 8 + wid;
  const char* kbase = ws + WS_K + (size_t)hd * NT32 * 2048; const char* vbase = ws + WS_V + (size_t)hd * NT32 * 2048;
  const unsigned ldsb = (unsigned)(uintptr_t)lds, l16 = lane * 16;
  const __amdgpu_buffer_rsrc_t rs = __builtin_amdgcn_make_buffer_rsrc(ws, 0, WS_END, 0x00020000);
#define GL2 glds16_sc1
#define P2_DMA1(e, t, st) GL2(((e) < 2 ? kbase : vbase) + (size_t)((r + (t)) & 7) * 16384 + wid * 2048 + ((e) & 1) * 1024, l16, ldsb + (st) + ((e) >> 1) * 16384 + wid * 2048 + ((e) & 1) * 1024)
  LAS const char* lp = lds + lane * 16;
  const bf16x8 q0 = *(LAS const bf16x8*)(lp + AT_Q + wid * 2048), q1 = *(LAS const bf16x8*)(lp + AT_Q + wid * 2048 + 1024);
  LAS const char* lp2 = lp + 2 * STAGE; asm volatile("" : "+v"(lp2));
#define LPS(st) ((st) >= 2 * STAGE ? lp2 - 2 * STAGE : lp)
#define KFR(st, b, h) (*(LAS const bf16x8*)(LPS(st) + (st) + (b) * 2048 + (h) * 1024))
#define VFR(st, b, h) (*(LAS const bf16x8*)(LPS(st) + (st) + 16384 + (b) * 2048 + (h) * 1024))
  f32x16 negm;
  {
    float m = -INFINITY;
#pragma unroll
    for (int b = 0; b < 4; ++b) {
      f32x16 s = {}; s = MFMA32(KFR(0, b, 0), q0, s); s = MFMA32(KFR(0, b, 1), q1, s);
#pragma unroll
      for (int e = 0; e < 16; ++e) m = fmaxf(m, s[e]);
    }
    m = halfmax(m);
#pragma unroll
    for (int e = 0; e < 16; ++e) negm[e] = -m;
    PIN(negm);
  }
  bf16x8 ones; { const bool on = ((lane & 1) == ((lane >> 4) & 1)); const __bf16 o = on ? (__bf16)1.0f : (__bf16)0.0f;
#pragma unroll
    for (int e = 0; e < 8; ++e) ones[e] = o; }
  PIN(ones);
  f32x16 o = {}; f32x4 ls0 = {}, ls1 = {};
  f32x16 sA, sB; u32x4 pA0, pA1, pB0 = {}, pB1 = {}; bf16x8 kA0, kA1, kB0, kB1, vA0, vA1, vB0 = {}, vB1 = {};
  sA = MFMA32(KFR(0, 0, 0), q0, negm); sA = MFMA32(KFR(0, 0, 1), q1, sA);
  kB0 = KFR(0, 1, 0); kB1 = KFR(0, 1, 1);
  STAMP_AT(1);
#define EX(S, i) S[i] = __builtin_amdgcn_exp2f(S[i])
#define PK(S, i) cvtpk(S[i], S[i + 1])
#define BF(x) __builtin_bit_cast(bf16x8, x)
#define P2_STEP(SX, SY, PX0, PX1, PY0, PY1, KX0, KX1, KY0, KY1, VX0, VX1, VY0, VY1, kst, kb, vst, vb, DMA) do { \
    SY = MFMA32(KY0, q0, negm); SBAR(); VX0 = VFR(vst, vb, 0); EX(SX, 0); EX(SX, 1); EX(SX, 2); EX(SX, 3); PIN(SX); SBAR(); \
    o = MFMA32(VY0, BF(PY0), o); SBAR(); VX1 = VFR(vst, vb, 1); EX(SX, 4); EX(SX, 5); EX(SX, 6); PX0[0] = PK(SX, 0); PX0[1] = PK(SX, 2); PIN(SX); PIN(PX0); SBAR(); \
    ls0 = MFMA16(ones, BF(PY0), ls0); SBAR(); EX(SX, 7); SBAR(); PX0[2] = PK(SX, 4); SBAR(); PX0[3] = PK(SX, 6); PIN(PX0); SBAR(); \
    SY = MFMA32(KY1, q1, SY); SBAR(); KX0 = KFR(kst, kb, 0); EX(SX, 8); EX(SX, 9); EX(SX, 10); EX(SX, 11); PIN(SX); SBAR(); \
    o = MFMA32(VY1, BF(PY1), o); SBAR(); KX1 = KFR(kst, kb, 1); EX(SX, 12); EX(SX, 13); EX(SX, 14); PX1[0] = PK(SX, 8); PX1[1] = PK(SX, 10); PIN(SX); PIN(PX1); SBAR(); \
    ls1 = MFMA16(ones, BF(PY1), ls1); SBAR(); EX(SX, 15); SBAR(); PX1[2] = PK(SX, 12); SBAR(); PX1[3] = PK(SX, 14); PIN(PX1); DMA; SBAR(); } while (0)
#define STEP_A(kst, kb, vst, vb, DMA) P2_STEP(sA, sB, pA0, pA1, pB0, pB1, kA0, kA1, kB0, kB1, vA0, vA1, vB0, vB1, kst, kb, vst, vb, DMA)
#define STEP_B(kst, kb, vst, vb, DMA) P2_STEP(sB, sA, pB0, pB1, pA0, pA1, kB0, kB1, kA0, kA1, vB0, vB1, vA0, vA1, kst, kb, vst, vb, DMA)
  constexpr int D = 4;
  const int tm = (xcd >> 1) * 4 + (i & 3), tn = (xcd & 1) * 8 + (i >> 2);
  const unsigned ab = WS_A + (unsigned)(tm * 4) * KS_A * 1024, wb = WS_W + (unsigned)tn * KS_A * 2048;
  bf16x8 a[D][4], wf[D][2];
  auto load = [&](int slot, int s) {
    const unsigned ks = (unsigned)((s < 28 ? KS_O + 8 * s : 8 * (s - 28)) + wid);
#pragma unroll
    for (int u = 0; u < 4; ++u) a[slot][u] = __builtin_bit_cast(bf16x8, __builtin_amdgcn_raw_buffer_load_b128(rs, l16, ab + (u * KS_A + ks) * 1024, SC1));
#pragma unroll
    for (int b = 0; b < 2; ++b) wf[slot][b] = __builtin_bit_cast(bf16x8, __builtin_amdgcn_raw_buffer_load_b128(rs, l16, wb + ks * 2048 + b * 1024, SC1));
  };
  gu64* const pkv = (gu64*)(ws + WS_FKV) + hd * 32 + (lane & 31);
  gu64* const pp1 = (gu64*)(ws + WS_FP1) + (lane < 32 ? r : (tm >> 1)) * 32 + (lane & 31);
  gu64* const pfw = (gu64*)(ws + WS_FW) + lane;
  u64 fkv = 0, f0 = 0, f1 = 0, f2 = 0, f3 = 0, f4 = 0, g2 = 0; unsigned gqu = 0;
#pragma unroll
  for (int t = 0; t < NTILE; ++t) {
    const unsigned st = (t % 3) * STAGE, sn = ((t + 1) % 3) * STAGE, sp = ((t + 2) % 3) * STAGE;
    STEP_A(st, 2, st, 0, if (t >= 2 && t + 1 < NTILE) P2_DMA1(1, t + 1, sn)); STEP_B(st, 3, st, 1, if (t >= 2 && t + 1 < NTILE) P2_DMA1(3, t + 1, sn));
    STEP_A(st, 4, st, 2, NOP); STEP_B(st, 5, st, 3, NOP); STEP_A(st, 6, st, 4, NOP); STEP_B(st, 7, st, 5, NOP);
    if (t == 0) { if (wid < 4) asm volatile("s_waitcnt vmcnt(13)\n\ts_barrier" ::: "memory"); else asm volatile("s_waitcnt vmcnt(19)\n\ts_barrier" ::: "memory");
      fkv = __hip_atomic_load(pkv, RLX_AGENT); SBAR(); }
    else if (t == 1) {
      WAIT_VM(0); flag_poll(fkv, pkv, epoch);
      asm volatile("s_barrier" ::: "memory"); }
    else {
      WAIT_VM(0);
      if (t == 6) { flag_poll(f0, pp1, epoch); flag_poll(f1, pfw, epoch); flag_poll(f2, pfw + 64, epoch); flag_poll(f3, pfw + 128, epoch); flag_poll(f4, pfw + 192, epoch); }
      asm volatile("s_barrier" ::: "memory");
    }
    if (t == 1 && threadIdx.x == 0) flag_set(ws, WS_FP1 + (unsigned)(r * 32 + hd) * 8, epoch);
    if (t == 2) {
      const int hi = lane >> 5, q4 = (lane & 15) >> 2, p4 = lane & 3, h16 = (lane >> 4) & 1;
      LAS const char* tp = lds + AT_WT + (4 * hi + q4) * 64 + 32 * h16 + 8 * p4;
#pragma unroll
      for (int m = 0; m < 5; ++m) if (wid + 8 * m < 36) { const int f = 36 * w + wid + 8 * m, ks = f >> 5, fb = f & 31;
        const bf16x8 v = tr_frag(tp + (wid + 8 * m) * 1024, tp + (wid + 8 * m) * 1024 + 8 * 64);
        st16(rs, l16, WS_W + (unsigned)(((fb >> 1) * KS_A + ks) * 2 + (fb & 1)) * 1024, __builtin_bit_cast(u32x4, v)); }
      SBAR();
    }
    if (t == 3 && threadIdx.x == 0) flag_set(ws, WS_FW + (unsigned)w * 8, epoch);
    if (t == 5) { f0 = __hip_atomic_load(pp1, RLX_AGENT); f1 = __hip_atomic_load(pfw, RLX_AGENT); f2 = __hip_atomic_load(pfw + 64, RLX_AGENT); f3 = __hip_atomic_load(pfw + 128, RLX_AGENT); f4 = __hip_atomic_load(pfw + 192, RLX_AGENT); SBAR(); }
    if (t == 6) {
      gqu = __hip_atomic_load((GAS unsigned*)(ws + WS_G + (T * 32 + (lane & 31)) * 4), RLX_AGENT);
      g2 = __hip_atomic_load((gu64*)(ws + WS_G + (tm * 128 + lane * 2) * 4), RLX_AGENT);
      SBAR();
    }
    if (t == 7) {
#pragma unroll
      for (int s = 0; s < D; ++s) load(s, s);
      SBAR();
    }
    STEP_A(sn, 0, st, 6, if (t >= 1 && t + 2 < NTILE) P2_DMA1(0, t + 2, sp)); STEP_B(sn, 1, st, 7, if (t >= 1 && t + 2 < NTILE) P2_DMA1(2, t + 2, sp));
  }
  o = MFMA32(vB0, BF(pB0), o); o = MFMA32(vB1, BF(pB1), o);
  ls0 = MFMA16(ones, BF(pB0), ls0); ls1 = MFMA16(ones, BF(pB1), ls1);
  STAMP_AT(2);
  const float gq = __uint_as_float(gqu);

  float l = (lane & 16) ? ls0[1] + ls1[1] : ls0[0] + ls1[0];
  float amax = 0.f;
#pragma unroll
  for (int e = 0; e < 16; ++e) amax = fmaxf(amax, fabsf(o[e]));
  const bool bad = !(l < 1.0995116e12f) || !(amax < INFINITY);
  if (__builtin_expect(__any(bad), 0)) {
    float m = -INFINITY; l = 0.f;
#pragma unroll
    for (int e = 0; e < 16; ++e) o[e] = 0.f;
    for (int b = 0; b < NT32; ++b) {
      const unsigned ko = WS_K + (unsigned)(hd * NT32 + b) * 2048, vo = WS_V + (unsigned)(hd * NT32 + b) * 2048;
      const bf16x8 k0 = BF(__builtin_amdgcn_raw_buffer_load_b128(rs, l16, ko, SC1)), k1 = BF(__builtin_amdgcn_raw_buffer_load_b128(rs, l16, ko + 1024, SC1));
      const bf16x8 v0 = BF(__builtin_amdgcn_raw_buffer_load_b128(rs, l16, vo, SC1)), v1 = BF(__builtin_amdgcn_raw_buffer_load_b128(rs, l16, vo + 1024, SC1));
      f32x16 s = {}; s = MFMA32(k0, q0, s); s = MFMA32(k1, q1, s);
      float mb = s[0];
#pragma unroll
      for (int e = 1; e < 16; ++e) mb = fmaxf(mb, s[e]);
      const float mn = fmaxf(m, halfmax(mb)); const float al = __builtin_amdgcn_exp2f(m - mn); m = mn;
#pragma unroll
      for (int e = 0; e < 16; ++e) { s[e] = __builtin_amdgcn_exp2f(s[e] - mn); o[e] *= al; }
      const u32x4 w0 = pack8(s, 0, 1.f), w1 = pack8(s, 8, 1.f);
      float rsum = 0.f;
#pragma unroll
      for (int e = 0; e < 4; ++e) rsum += __uint_as_float(w0[e] << 16) + __uint_as_float(w0[e] & 0xffff0000u) + __uint_as_float(w1[e] << 16) + __uint_as_float(w1[e] & 0xffff0000u);
      { auto rr = __builtin_amdgcn_permlane32_swap(__float_as_uint(rsum), __float_as_uint(rsum), false, false); rsum = __uint_as_float(rr[0]) + __uint_as_float(rr[1]); }
      l = l * al + rsum;
      o = MFMA32(v0, BF(w0), o); o = MFMA32(v1, BF(w1), o);
    }
  }
  const float sc = gq / l;
  const unsigned oo = WS_A + (unsigned)(T * KS_A + hd * 2) * 1024;
  st16(rs, l16, oo, pack8(o, 0, sc)); st16(rs, l16, oo + 1024, pack8(o, 8, sc));
  LAS float* gl = (LAS float*)(lds + P3_G + wid * 512);
  *(LAS f32x2*)(gl + lane * 2) = (f32x2){1.f - __uint_as_float((unsigned)g2), 1.f - __uint_as_float((unsigned)(g2 >> 32))};
  WAITVM_BAR(0);
  if (threadIdx.x == 0) flag_set(ws, WS_FO + (unsigned)(r * 32 + hd) * 8, epoch);
  STAMP_AT(3); STAMP_END();

  STAMP_NEXT(); STAMP_AT(0);
  const int l31 = lane & 31;
  f32x16 acc[4][2];
#pragma unroll
  for (int u = 0; u < 4; ++u)
#pragma unroll
    for (int b = 0; b < 2; ++b)
#pragma unroll
      for (int e = 0; e < 16; ++e) acc[u][b][e] = 0.f;
  auto mma = [&](int d) {
#pragma unroll
    for (int u = 0; u < 4; ++u)
#pragma unroll
      for (int b = 0; b < 2; ++b) acc[u][b] = MFMA32(wf[d][b], a[d][u], acc[u][b]); };
#pragma unroll 1
  for (int so = 0; so < 28 - D; so += D) {
#pragma unroll
    for (int d = 0; d < D; ++d) { mma(d); SBAR(); load(d, so + d + D); SBAR(); }
  }
  flags_wait((gu64*)(ws + WS_FO) + (tm >> 1) * 32 + (lane & 31), epoch);
#pragma unroll
  for (int d = 0; d < D; ++d) { mma(d); SBAR(); load(d, 28 + d); SBAR(); }
#pragma unroll
  for (int u = 0; u < 4; ++u) { const float gt = gl[u * 32 + l31];
#pragma unroll
    for (int b = 0; b < 2; ++b)
#pragma unroll
      for (int e = 0; e < 16; ++e) acc[u][b][e] *= gt;
    SBAR(); }
#pragma unroll
  for (int d = 0; d < D; ++d) { mma(d); SBAR(); load(d, 32 + d); SBAR(); }
#pragma unroll
  for (int d = 0; d < D; ++d) mma(d);
  STAMP_AT(1);
  int tz = threadIdx.x; asm volatile("" : "+v"(tz));
  const int ln = tz & 63;
  LAS f32x4* red = (LAS f32x4*)lds + ln;
  f32x4 res[4];
#pragma unroll
  for (int ph = 0; ph < 2; ++ph) {
    if (ph) __syncthreads();
#pragma unroll
    for (int u = 0; u < 4; ++u)
#pragma unroll
      for (int b = 0; b < 2; ++b)
#pragma unroll
        for (int e = 0; e < 2; ++e) { const int oi = 8 * ph + 4 * e;
          red[((wid * 8 + u * 2 + b) * 2 + e) * 64] = (f32x4){acc[u][b][oi], acc[u][b][oi + 1], acc[u][b][oi + 2], acc[u][b][oi + 3]}; }
    __syncthreads();
    if (ph == 0) STAMP_AT(2);
#pragma unroll
    for (int e = 0; e < 2; ++e) { f32x4 s = red[((0 * 8 + wid) * 2 + e) * 64];
#pragma unroll
      for (int g = 1; g < 8; ++g) s += red[((g * 8 + wid) * 2 + e) * 64];
      res[2 * ph + e] = s; }
  }
  {
    unsigned pk[4][2];
#pragma unroll
    for (int e = 0; e < 4; ++e) { pk[e][0] = cvtpk(res[e][0], res[e][1]); pk[e][1] = cvtpk(res[e][2], res[e][3]); }
    const __amdgpu_buffer_rsrc_t ro = __builtin_amdgcn_make_buffer_rsrc(out, 0, SEQ * DM * 2, 0x00020000);
    const unsigned ob = (unsigned)((tm * 128 + (wid >> 1) * 32 + (ln & 31)) * 2048 + (tn * 64 + (wid & 1) * 32) * 2 + (ln >> 5) * 16);
#pragma unroll
    for (int e = 0; e < 4; e += 2) {
      const auto r0 = __builtin_amdgcn_permlane32_swap(pk[e][0], pk[e + 1][0], false, false), r1 = __builtin_amdgcn_permlane32_swap(pk[e][1], pk[e + 1][1], false, false);
      const u32x4 v = {r0[0], r1[0], r0[1], r1[1]};
      __builtin_amdgcn_raw_buffer_store_b128(v, ro, ob + e * 16, 0, OUT_AUX);
    }
  }
  STAMP_AT(3); STAMP_END();
}

__global__ __launch_bounds__(512, 2) void mega(const char* x, const char* wqkv, const char* wo, const char* wg, const char* wu, const char* wd, const char* gatew, char* ws, char* out) {
  extern __shared__ __attribute__((aligned(16))) char smem[];
  LAS char* lds = (LAS char*)smem;
  const int tid = threadIdx.x, lane = tid & 63, wid = __builtin_amdgcn_readfirstlane(tid >> 6);
  const int w = blockIdx.x, xcd = w & 7, i = w >> 3, j = xcd * 4 + (i & 3), r = i >> 2;
  u64 epoch;
  if (wid < 4) epoch = phase1<0>(lds, x, wqkv, wo, wg, wu, wd, gatew, ws, out, w, j, r, wid, lane); else epoch = phase1<1>(lds, x, wqkv, wo, wg, wu, wd, gatew, ws, out, w, j, r, wid, lane);
  phase23(lds, ws, out, epoch, w, j, r, xcd, i, wid, lane);
  if (w == 0 && tid == 0) flag_set(ws, WS_E, epoch);
}

extern "C" void kernel_launch(void* const* d_in, const int* in_sizes, int n_in, void* d_out, int out_size, void* d_ws, size_t ws_size, hipStream_t stream) {
  (void)hipFuncSetAttribute((const void*)mega, hipFuncAttributeMaxDynamicSharedMemorySize, lm::BYTES);
  mega<<<256, 512, lm::BYTES, stream>>>((const char*)d_in[0], (const char*)d_in[1], (const char*)d_in[2], (const char*)d_in[4], (const char*)d_in[5], (const char*)d_in[6],
                                        (const char*)d_in[7], (char*)d_ws, (char*)d_out);
}
```

```cpp
#include <hip/hip_runtime.h>
#include <stdint.h>

typedef __bf16 bf16x8 __attribute__((ext_vector_type(8)));
typedef __bf16 bf16x2 __attribute__((ext_vector_type(2)));
typedef short s16x4 __attribute__((ext_vector_type(4)));
typedef float f32x16 __attribute__((ext_vector_type(16)));
typedef float f32x4 __attribute__((ext_vector_type(4)));
typedef float f32x2 __attribute__((ext_vector_type(2)));
typedef unsigned u32x4 __attribute__((ext_vector_type(4)));
typedef unsigned u32x2 __attribute__((ext_vector_type(2)));
typedef unsigned long long u64;
#define LAS __attribute__((address_space(3)))
#define GAS __attribute__((address_space(1)))
#define DI __device__ __forceinline__
#define SBAR() __builtin_amdgcn_sched_barrier(0)
#define PIN(x) asm volatile("" : "+v"(x))
#define MFMA32(a, b, c) __builtin_amdgcn_mfma_f32_32x32x16_bf16((a), (b), (c), 0, 0, 0)
#define MFMA16(a, b, c) __builtin_amdgcn_mfma_f32_16x16x32_bf16((a), (b), (c), 0, 0, 0)
#define RLX_AGENT __ATOMIC_RELAXED, __HIP_MEMORY_SCOPE_AGENT
#define SC1 16
#define OUT_AUX 0

constexpr int SEQ = 2048, DM = 1024, FF = 3584, NH = 32;
constexpr int NT32 = SEQ / 32;
constexpr int KS_O = DM / 16;
constexpr int KS_H = FF / 16;
constexpr int KS_A = KS_O + KS_H;
constexpr unsigned WS_E = 0;
constexpr unsigned WS_FKV = 256;
constexpr unsigned WS_FP1 = WS_FKV + 32 * 32 * 8;
constexpr unsigned WS_FW = WS_FP1 + 256 * 8;
constexpr unsigned WS_FO = WS_FW + 256 * 8;
constexpr unsigned WS_G = 16384;
constexpr unsigned WS_K = 32768;
constexpr unsigned WS_V = WS_K + NH * NT32 * 2048;
constexpr unsigned WS_A = WS_V + NH * NT32 * 2048;
constexpr unsigned WS_W = WS_A + NT32 * KS_A * 1024;
constexpr unsigned WS_END = WS_W + 16 * KS_A * 2048;
static_assert(WS_FO + 256 * 8 <= WS_G, "flags");

DI unsigned cvtpk(float lo, float hi) { f32x2 v = {lo, hi}; bf16x2 b = __builtin_convertvector(v, bf16x2); return __builtin_bit_cast(unsigned, b); }
DI u32x4 pack8(const f32x16& a, int o, float s) {
  u32x4 w; w[0] = cvtpk(a[o + 0] * s, a[o + 1] * s); w[1] = cvtpk(a[o + 2] * s, a[o + 3] * s); w[2] = cvtpk(a[o + 4] * s, a[o + 5] * s); w[3] = cvtpk(a[o + 6] * s, a[o + 7] * s); return w; }
DI bf16x8 tr_frag(LAS const char* p0, LAS const char* p1) {
  s16x4 lo = __builtin_amdgcn_ds_read_tr16_b64_v4i16((LAS s16x4*)p0);
  s16x4 hi = __builtin_amdgcn_ds_read_tr16_b64_v4i16((LAS s16x4*)p1);
  return __builtin_bit_cast(bf16x8, __builtin_shufflevector(lo, hi, 0, 1, 2, 3, 4, 5, 6, 7)); }
DI void glds16(const char* sbase, unsigned voff, unsigned ldst) {
  asm volatile("s_mov_b32 m0, %0\n\ts_nop 0\n\tglobal_load_lds_dwordx4 %1, %2" ::"s"(ldst), "v"(voff), "s"(sbase) : "memory"); }
DI void glds16_sc1(const char* sbase, unsigned voff, unsigned ldst) {
  asm volatile("s_mov_b32 m0, %0\n\ts_nop 0\n\tglobal_load_lds_dwordx4 %1, %2 sc1" ::"s"(ldst), "v"(voff), "s"(sbase) : "memory"); }
DI void glds16c(int on, const char* sbase, unsigned voff, unsigned ldst) {
  asm volatile("s_cmp_lt_i32 %3, 1\n\ts_cbranch_scc1 1f\n\ts_mov_b32 m0, %0\n\ts_nop 0\n\tglobal_load_lds_dwordx4 %1, %2\n1:" ::"s"(ldst), "v"(voff), "s"(sbase), "s"(on) : "memory", "scc"); }
DI void l2_prefetch(const char* sbase, unsigned voff, unsigned ldst) {
  asm volatile("s_mov_b32 m0, %0\n\ts_nop 0\n\tglobal_load_lds_dword %1, %2 sc1" ::"s"(ldst), "v"(voff), "s"(sbase) : "memory"); }
#define STAMP_NEXT()
#define STAMP_DECL(kid)
#define STAMP_AT(i)
#define STAMP_END()
#define WAIT_VM(n) asm volatile("s_waitcnt vmcnt(" #n ")" ::: "memory")
#define WAITVM_BAR(n) asm volatile("s_waitcnt vmcnt(" #n ") lgkmcnt(0)\n\ts_barrier" ::: "memory")

typedef GAS u64 gu64;
DI void flag_set(char* ws, unsigned off, u64 epoch) { __hip_atomic_store((gu64*)(ws + off), epoch, RLX_AGENT); }
DI void flag_poll(u64 v, const gu64* p, u64 epoch) {
  unsigned n;
  asm volatile("s_mov_b32 %1, 0\n1:\n\tv_cmp_ne_u64 vcc, %2, %0\n\ts_cbranch_vccz 2f\n\ts_add_u32 %1, %1, 1\n\ts_cmp_gt_u32 %1, 0xffff\n\ts_cbranch_scc1 2f\n\ts_sleep 1\n\t"
               "global_load_dwordx2 %0, %3, off sc1\n\ts_waitcnt vmcnt(0)\n\ts_branch 1b\n2:"
               : "+v"(v), "=&s"(n) : "s"(epoch), "v"(p) : "vcc", "scc", "memory");
}
DI void flags_wait(const gu64* p, u64 epoch) { flag_poll(__hip_atomic_load(p, RLX_AGENT), p, epoch); }
DI void st16(__amdgpu_buffer_rsrc_t rs, unsigned voff, unsigned soff, u32x4 v) { __builtin_amdgcn_raw_buffer_store_b128(v, rs, voff + soff, 0, SC1); }

namespace lm {
constexpr int P1_X = 0, P1_QA = 32768, P1_QB = 38912, P1_WG = 45056, P1_WU = 59392, P1_STAGE = 73728, P1_PF = 2 * P1_STAGE;
constexpr int AT_STAGE = 32768, AT_WT = 3 * AT_STAGE, AT_Q = 139264;
constexpr int P3_G = 131072, BYTES = 163840;
}

constexpr float QSCALE = 0.17677669529663687f * 1.4426950408889634f;
template <int WC> DI u64 phase1(u32x4 (&hw)[10], LAS char* lds, const char* x, const char* wqkv, const char* wo, const char* wg, const char* wu, const char* wd, const char* gatew, char* ws, int w, int j, int r, int wid, int lane) {
  using namespace lm;
  STAMP_DECL(0); STAMP_AT(0);
  const int wr = wid & 3, g4 = lane >> 4, c16 = lane & 15;
  const unsigned ldsb = (unsigned)(uintptr_t)lds;
  const char* xsrc = x + (size_t)(r * 256 + wid * 32) * 2048;
  unsigned xoff[2];
#pragma unroll
  for (int e = 0; e < 2; ++e) xoff[e] = (unsigned)((lane >> 3) * 2048 + (((lane & 7) ^ ((4 * e + (lane >> 4)) & 7)) << 4));
  const char* wsrc[5]; unsigned woff[5], wdst[5], wstep[5];
#pragma unroll
  for (int m = 0; m < 5; ++m) {
    const int p = wid + 8 * m; const bool isq = p < 12, isa = p < 6, isg = p < 26;
    const int pp = isq ? (isa ? p : p - 6) : (isg ? p - 12 : p - 26);
    wsrc[m] = isq ? wqkv : (isg ? wg : wu);
    wdst[m] = (unsigned)__builtin_amdgcn_readfirstlane((isq ? (isa ? P1_QA : P1_QB) : (isg ? P1_WG : P1_WU)) + pp * 1024);
    wstep[m] = isq ? 64 * 6144 : 64 * 7168;
    const int cc = 64 * pp + lane, rq = cc / 6, pq = cc % 6, rg = cc / 14, pg = cc % 14;
    const int colq = isa ? (pq < 4 ? pq * 8 : 1024 + (pq - 4) * 8) : (pq < 2 ? 1024 + (2 + pq) * 8 : 2048 + (pq - 2) * 8);
    woff[m] = isq ? (unsigned)((rq ^ ((rq >> 1) & 4)) * 6144 + (colq + j * 32) * 2) : (unsigned)((rg ^ ((rg >> 1) & 4)) * 7168 + (j * 112 + pg * 8) * 2);
  }
#define GLC glds16c
#define P1_DMA_X(on, q, t, st) GLC(on, xsrc + (size_t)(t) * 128 + (q) * 8 * 2048, xoff[(q) & 1], ldsb + (st) + P1_X + (wid * 4 + (q)) * 1024)
#define P1_DMA_W(on, m, t, st) GLC(on, wsrc[m] + (size_t)(t) * wstep[m], woff[m], ldsb + (st) + wdst[m])
  STAMP_AT(6);
#pragma unroll
  for (int q = 0; q < 4; ++q) P1_DMA_X(1, q, 0, 0);
#pragma unroll
  for (int m = 0; m < 5; ++m) P1_DMA_W(1, m, 0, 0);
  STAMP_AT(7);
  asm volatile("s_barrier" ::: "memory");
  STAMP_AT(8);
#pragma unroll
  for (int q = 0; q < 4; ++q) P1_DMA_X(1, q, 1, P1_STAGE);
#pragma unroll
  for (int m = 0; m < 5; ++m) P1_DMA_W(1, m, 1, P1_STAGE);
  unsigned pfoff, pfstep;
  {
    const int jj = j & 3, xg = j >> 2, row = (r * 4 + jj) * 2 + (wid & 1), l6 = lane % 6, l7 = (lane & 31) % 7;
    const unsigned ox = (unsigned)((r * 256 + jj * 64 + wid * 16 + (lane & 15)) * 2048);
    const unsigned oq = (unsigned)(row * 6144 + (l6 >> 1) * 2048 + xg * 256 + (l6 & 1) * 128), og = (unsigned)(row * 7168 + xg * 896 + l7 * 128);
    pfoff = wid < 4 ? ox : (wid < 6 ? oq : og); pfstep = wid < 4 ? 128 : (wid < 6 ? 64 * 6144 : 64 * 7168);
  }
  const char* pfl = (wid >= 6 ? (lane < 32 ? wg : wu) : (wid < 4 ? x : wqkv)) + pfoff;
  const bool pfw = wid == 0 && lane >= 16 && lane < 34;
  if (pfw) { const int f = 36 * w + 2 * (lane - 16), ks = f >> 5, fb = f & 31;
    pfl = (ks < KS_O ? wo + (size_t)ks * 16 * 2048 : wd + (size_t)(ks - KS_O) * 16 * 2048) + fb * 64; pfstep = 2048; }
  auto pf = [&](int t, int tw) { asm volatile("s_mov_b32 m0, %0\n\ts_nop 0\n\tglobal_load_lds_dword %1, off sc1" ::"s"(ldsb + P1_PF), "v"(pfl + (size_t)(unsigned)((pfw ? tw : t) * pfstep)) : "memory"); };
  unsigned xa[2];
#pragma unroll
  for (int ks = 0; ks < 2; ++ks) xa[ks] = (unsigned)(P1_X + (wr * 64 + c16) * 128 + (((4 * ks + g4) ^ ((c16 >> 1) & 7)) << 4));
  unsigned qa[2], ga[2];
#pragma unroll
  for (int t = 0; t < 2; ++t) { const int prow = 8 * g4 + 4 * (t ^ (g4 & 1)) + (c16 >> 2);
    qa[t] = (unsigned)(P1_QA + prow * 96 + 8 * (c16 & 3)); ga[t] = (unsigned)(P1_WG + prow * 224 + 8 * (c16 & 3)); }
  f32x4 acc[4][10];
#pragma unroll
  for (int u = 0; u < 4; ++u)
#pragma unroll
    for (int b = 0; b < 10; ++b) acc[u][b] = (f32x4){0.f, 0.f, 0.f, 0.f};
  bf16x8 xf[2][4]; s16x4 wlo[4], whi[4];
  auto ldx = [&](int u, unsigned st, int ks) { xf[ks][u] = *(LAS const bf16x8*)(lds + st + xa[ks] + u * 2048); };
  auto ldw = [&](int b, unsigned st, int ks, int h) {
    const int slot = (10 * ks + b) & 3;
    unsigned a;
    if (WC == 0 && b < 6) a = qa[h] + ks * 32 * 96 + (b < 3 ? b * 32 : (P1_QB - P1_QA) + (b - 3) * 32);
    else { const int gb = WC == 0 ? (b - 6) >> 1 : 2 + (b >> 1); a = ga[h] + ks * 32 * 224 + gb * 32 + ((b & 1) ? P1_WU - P1_WG : 0); }
    const s16x4 v = __builtin_amdgcn_ds_read_tr16_b64_v4i16((LAS s16x4*)(lds + st + a));
    if (h == 0) wlo[slot] = v; else whi[slot] = v; };
  auto mma1 = [&](int b, int ks, int u) {
    const int slot = (10 * ks + b) & 3;
    const bf16x8 wf = __builtin_bit_cast(bf16x8, __builtin_shufflevector(wlo[slot], whi[slot], 0, 1, 2, 3, 4, 5, 6, 7));
    if (WC == 0 && (b == 4 || b == 5)) acc[u][b] = MFMA16(xf[ks][u], wf, acc[u][b]); else acc[u][b] = MFMA16(wf, xf[ks][u], acc[u][b]); };
#define NOP (void)0
  pf(2, 0); pf(3, 0);
  const u64 e0 = __hip_atomic_load((gu64*)(ws + WS_E), RLX_AGENT);
  l2_prefetch(gatew, (unsigned)(lane & 15) * 128, ldsb + P1_PF);
  STAMP_AT(9);
  asm volatile("s_waitcnt vmcnt(13)\n\ts_barrier" ::: "memory");
  STAMP_AT(1);
#pragma unroll
  for (int u = 0; u < 4; ++u) ldx(u, 0, 0);
#pragma unroll
  for (int b = 0; b < 3; ++b) { ldw(b, 0, 0, 0); ldw(b, 0, 0, 1); }
  unsigned cur = 0;
#pragma unroll 1
  for (int t = 0; t < 16; ++t) {
    const unsigned nxt = P1_STAGE - cur;
    const int m1 = t * (15 - t), m2 = 14 - t;
    const int tp = t + 4 < 16 ? t + 4 : 15;
#define P1_GRP(b, ks, R0, R1, R2, R3) do { mma1(b, ks, 0); SBAR(); R0; SBAR(); mma1(b, ks, 1); SBAR(); R1; SBAR(); mma1(b, ks, 2); SBAR(); R2; SBAR(); mma1(b, ks, 3); SBAR(); R3; SBAR(); } while (0)
#define LDW_AHEAD0(b, h) do { if ((b) + 3 < 10) ldw((b) + 3, cur, 0, h); else ldw((b) + 3 - 10, cur, 1, h); } while (0)
#define P1_G0(b, SIDE) P1_GRP(b, 0, LDW_AHEAD0(b, 0), LDW_AHEAD0(b, 1), if ((b) < 4) ldx(b, cur, 1), SIDE)
    P1_G0(0, P1_DMA_X(m1, 3, t + 1, nxt)); P1_G0(1, P1_DMA_W(m1, 0, t + 1, nxt)); P1_G0(2, P1_DMA_W(m1, 1, t + 1, nxt)); P1_G0(3, P1_DMA_W(m1, 2, t + 1, nxt));
    P1_G0(4, P1_DMA_W(m1, 3, t + 1, nxt)); P1_G0(5, P1_DMA_W(m1, 4, t + 1, nxt)); P1_G0(6, pf(tp, t)); P1_G0(7, NOP); P1_G0(8, NOP); P1_G0(9, NOP);
#define P1_G1(b) P1_GRP(b, 1, ldw((b) + 3, cur, 1, 0), ldw((b) + 3, cur, 1, 1), NOP, NOP)
    P1_G1(0); P1_G1(1); P1_G1(2); P1_G1(3); P1_G1(4); P1_G1(5); P1_G1(6);
    asm volatile("s_cmp_lg_u32 %0, 0\n\ts_cbranch_scc1 1f\n\ts_waitcnt vmcnt(5) lgkmcnt(0)\n\ts_branch 2f\n1:\n\ts_waitcnt vmcnt(1) lgkmcnt(0)\n2:\n\ts_barrier" ::"s"(t) : "memory", "scc");
    P1_GRP(7, 1, ldx(0, nxt, 0); ldw(0, nxt, 0, 0), ldx(1, nxt, 0); ldw(0, nxt, 0, 1), ldx(2, nxt, 0), ldx(3, nxt, 0); P1_DMA_X(m2, 0, t + 2, cur));
    P1_GRP(8, 1, ldw(1, nxt, 0, 0), ldw(1, nxt, 0, 1), NOP, P1_DMA_X(m2, 1, t + 2, cur));
    P1_GRP(9, 1, ldw(2, nxt, 0, 0), ldw(2, nxt, 0, 1), NOP, P1_DMA_X(m2, 2, t + 2, cur));
    cur = nxt;
  }
  WAITVM_BAR(0);
  STAMP_AT(2);
  const u64 epoch = (((u64)(unsigned)__builtin_amdgcn_readfirstlane((unsigned)(e0 >> 32)) << 32) | (unsigned)__builtin_amdgcn_readfirstlane((unsigned)e0)) + 1;
  const __amdgpu_buffer_rsrc_t rs = __builtin_amdgcn_make_buffer_rsrc(ws, 0, WS_END, 0x00020000);
  const unsigned l16 = lane * 16;
  const int gtok = r * 256 + j * 8 + wid;
  const u32x4* gxp = (const u32x4*)(x + (size_t)gtok * 2048) + lane * 2; const u32x4* ggp = (const u32x4*)gatew + lane * 2;
  const u32x4 ga0 = gxp[0], ga1 = gxp[1], gb0 = ggp[0], gb1 = ggp[1];
  SBAR();
  auto frag16 = [&](const f32x4& lo, const f32x4& hi, float s) {
    const auto r0 = __builtin_amdgcn_permlane16_swap(cvtpk(lo[0] * s, lo[1] * s), cvtpk(hi[0] * s, hi[1] * s), false, false);
    const auto r1 = __builtin_amdgcn_permlane16_swap(cvtpk(lo[2] * s, lo[3] * s), cvtpk(hi[2] * s, hi[3] * s), false, false);
    return (u32x4){r0[0], r1[0], r0[1], r1[1]}; };
  if (WC == 0) {
    const unsigned vl16 = (unsigned)(((g4 & 1) * 32 + (g4 >> 1) * 16 + c16) * 16);
#pragma unroll
    for (int tb = 0; tb < 2; ++tb) {
      const int tl = wr * 2 + tb, T = r * 8 + tl;
      const unsigned go = (unsigned)(j * NT32 + T) * 2048;
      LAS char* kl = lds + tl * 2048;
#pragma unroll
      for (int t = 0; t < 2; ++t) {
        const u32x4 kf = frag16(acc[2 * tb][2 + t], acc[2 * tb + 1][2 + t], 1.f);
        st16(rs, l16, WS_K + go + t * 1024, kf); *(LAS u32x4*)(kl + t * 1024 + l16) = kf;
        const f32x4 &v0 = acc[2 * tb + t][4], &v1 = acc[2 * tb + t][5];
        const auto s0 = __builtin_amdgcn_permlane32_swap(cvtpk(v0[0], v0[1]), cvtpk(v1[0], v1[1]), false, false);
        const auto s1 = __builtin_amdgcn_permlane32_swap(cvtpk(v0[2], v0[3]), cvtpk(v1[2], v1[3]), false, false);
        const u32x4 vf = {s0[0], s1[0], s0[1], s1[1]};
        st16(rs, vl16, WS_V + go + t * 1024, vf); *(LAS u32x4*)(kl + 16384 + t * 1024 + vl16) = vf;
        *(LAS u32x4*)(lds + AT_Q + tl * 2048 + t * 1024 + l16) = frag16(acc[2 * tb][t], acc[2 * tb + 1][t], QSCALE);
      }
    }
  }
  SBAR();
#pragma unroll
  for (int tb = 0; tb < 2; ++tb)
#pragma unroll
    for (int bb = 0; bb < 5; ++bb) {
      if (WC == 0 && bb < 3) { hw[tb * 5 + bb] = (u32x4){0u, 0u, 0u, 0u}; continue; }
      const int b = WC == 0 ? 2 * bb : 2 * bb;
      f32x4 h[2];
#pragma unroll
      for (int e = 0; e < 2; ++e)
#pragma unroll
        for (int i = 0; i < 4; ++i) { const float g = acc[2 * tb + e][b][i], up = acc[2 * tb + e][b + 1][i];
          h[e][i] = g * __builtin_amdgcn_rcpf(1.f + __builtin_amdgcn_exp2f(-1.4426950408889634f * g)) * up; }
      hw[tb * 5 + bb] = frag16(h[0], h[1], 1.f);
      PIN(hw[tb * 5 + bb]);
    }
  {
    float s = 0.f;
#pragma unroll
    for (int e = 0; e < 4; ++e) { const unsigned ua0 = ga0[e], ub0 = gb0[e], ua1 = ga1[e], ub1 = gb1[e];
      s = __builtin_amdgcn_fdot2_f32_bf16(__builtin_bit_cast(bf16x2, ua0), __builtin_bit_cast(bf16x2, ub0), s, false);
      s = __builtin_amdgcn_fdot2_f32_bf16(__builtin_bit_cast(bf16x2, ua1), __builtin_bit_cast(bf16x2, ub1), s, false); }
#pragma unroll
    for (int o = 32; o >= 1; o >>= 1) s += __shfl_xor(s, o);
    const float g = __builtin_amdgcn_rcpf(1.f + __builtin_amdgcn_exp2f(-1.4426950408889634f * s));
    __hip_atomic_store((GAS unsigned*)(ws + WS_G + gtok * 4), __float_as_uint(g), RLX_AGENT);
  }
  SBAR();
  if (WC == 0) {
    WAIT_VM(1);
    if (lane == 0) flag_set(ws, WS_FKV + (unsigned)((j * 8 + r) * 4 + wr) * 8, epoch);
  }
  STAMP_AT(3);
  if (WC == 0 && wid == 0) {
    gu64* p = (gu64*)(ws + WS_FKV) + (j * 8 + ((r + 1 + ((lane >> 2) & 1)) & 7)) * 4 + (lane & 3);
    flags_wait(p, epoch);
  }
  asm volatile("s_waitcnt lgkmcnt(0)\n\ts_barrier" ::: "memory");
  STAMP_AT(4);
  {
    const char* kbase = ws + WS_K + (size_t)j * NT32 * 2048; const char* vbase = ws + WS_V + (size_t)j * NT32 * 2048;
#pragma unroll
    for (int t = 1; t < 3; ++t)
#pragma unroll
      for (int e = 0; e < 4; ++e)
        glds16_sc1((e < 2 ? kbase : vbase) + (size_t)((r + t) & 7) * 16384 + wid * 2048 + (e & 1) * 1024, l16, ldsb + t * AT_STAGE + (e >> 1) * 16384 + wid * 2048 + (e & 1) * 1024);
  }
  SBAR();
  STAMP_AT(5); STAMP_END();
  return epoch;
}

DI float halfmax(float m) { auto rr = __builtin_amdgcn_permlane32_swap(__float_as_uint(m), __float_as_uint(m), false, false); return fmaxf(__uint_as_float(rr[0]), __uint_as_float(rr[1])); }
DI void phase23(const u32x4 (&hw)[10], LAS char* lds, const char* wo, const char* wd, char* ws, char* out, u64 epoch, int w, int hd, int r, int xcd, int i, int wid, int lane) {
  using namespace lm;
  constexpr int NTILE = 8, STAGE = AT_STAGE;
  STAMP_DECL(1); STAMP_AT(0);
  const int T = r * 8 + wid;
  const char* kbase = ws + WS_K + (size_t)hd * NT32 * 2048; const char* vbase = ws + WS_V + (size_t)hd * NT32 * 2048;
  const unsigned ldsb = (unsigned)(uintptr_t)lds, l16 = lane * 16;
  const __amdgpu_buffer_rsrc_t rs = __builtin_amdgcn_make_buffer_rsrc(ws, 0, WS_END, 0x00020000);
#define GL2 glds16_sc1
#define P2_DMA1(e, t, st) GL2(((e) < 2 ? kbase : vbase) + (size_t)((r + (t)) & 7) * 16384 + wid * 2048 + ((e) & 1) * 1024, l16, ldsb + (st) + ((e) >> 1) * 16384 + wid * 2048 + ((e) & 1) * 1024)
  LAS const char* lp = lds + lane * 16;
  const bf16x8 q0 = *(LAS const bf16x8*)(lp + AT_Q + wid * 2048), q1 = *(LAS const bf16x8*)(lp + AT_Q + wid * 2048 + 1024);
  LAS const char* lp2 = lp + 2 * STAGE; asm volatile("" : "+v"(lp2));
#define LPS(st) ((st) >= 2 * STAGE ? lp2 - 2 * STAGE : lp)
#define KFR(st, b, h) (*(LAS const bf16x8*)(LPS(st) + (st) + (b) * 2048 + (h) * 1024))
#define VFR(st, b, h) (*(LAS const bf16x8*)(LPS(st) + (st) + 16384 + (b) * 2048 + (h) * 1024))
  f32x16 negm;
  {
    float m = -INFINITY;
#pragma unroll
    for (int b = 0; b < 4; ++b) {
      f32x16 s = {}; s = MFMA32(KFR(0, b, 0), q0, s); s = MFMA32(KFR(0, b, 1), q1, s);
#pragma unroll
      for (int e = 0; e < 16; ++e) m = fmaxf(m, s[e]);
    }
    m = halfmax(m);
#pragma unroll
    for (int e = 0; e < 16; ++e) negm[e] = -m;
    PIN(negm);
  }
  bf16x8 ones; { const bool on = ((lane & 1) == ((lane >> 4) & 1)); const __bf16 o = on ? (__bf16)1.0f : (__bf16)0.0f;
#pragma unroll
    for (int e = 0; e < 8; ++e) ones[e] = o; }
  PIN(ones);
  f32x16 o = {}; f32x4 ls0 = {}, ls1 = {};
  f32x16 sA, sB; u32x4 pA0, pA1, pB0 = {}, pB1 = {}; bf16x8 kA0, kA1, kB0, kB1, vA0, vA1, vB0 = {}, vB1 = {};
  sA = MFMA32(KFR(0, 0, 0), q0, negm); sA = MFMA32(KFR(0, 0, 1), q1, sA);
  kB0 = KFR(0, 1, 0); kB1 = KFR(0, 1, 1);
  STAMP_AT(1);
#define EX(S, i) S[i] = __builtin_amdgcn_exp2f(S[i])
#define PK(S, i) cvtpk(S[i], S[i + 1])
#define BF(x) __builtin_bit_cast(bf16x8, x)
#define P2_STEP(SX, SY, PX0, PX1, PY0, PY1, KX0, KX1, KY0, KY1, VX0, VX1, VY0, VY1, kst, kb, vst, vb, DMA) do { \
    SY = MFMA32(KY0, q0, negm); SBAR(); VX0 = VFR(vst, vb, 0); EX(SX, 0); EX(SX, 1); EX(SX, 2); EX(SX, 3); PIN(SX); SBAR(); \
    o = MFMA32(VY0, BF(PY0), o); SBAR(); VX1 = VFR(vst, vb, 1); EX(SX, 4); EX(SX, 5); EX(SX, 6); PX0[0] = PK(SX, 0); PX0[1] = PK(SX, 2); PIN(SX); PIN(PX0); SBAR(); \
    ls0 = MFMA16(ones, BF(PY0), ls0); SBAR(); EX(SX, 7); SBAR(); PX0[2] = PK(SX, 4); SBAR(); PX0[3] = PK(SX, 6); PIN(PX0); SBAR(); \
    SY = MFMA32(KY1, q1, SY); SBAR(); KX0 = KFR(kst, kb, 0); EX(SX, 8); EX(SX, 9); EX(SX, 10); EX(SX, 11); PIN(SX); SBAR(); \
    o = MFMA32(VY1, BF(PY1), o); SBAR(); KX1 = KFR(kst, kb, 1); EX(SX, 12); EX(SX, 13); EX(SX, 14); PX1[0] = PK(SX, 8); PX1[1] = PK(SX, 10); PIN(SX); PIN(PX1); SBAR(); \
    ls1 = MFMA16(ones, BF(PY1), ls1); SBAR(); EX(SX, 15); SBAR(); PX1[2] = PK(SX, 12); SBAR(); PX1[3] = PK(SX, 14); PIN(PX1); DMA; SBAR(); } while (0)
#define STEP_A(kst, kb, vst, vb, DMA) P2_STEP(sA, sB, pA0, pA1, pB0, pB1, kA0, kA1, kB0, kB1, vA0, vA1, vB0, vB1, kst, kb, vst, vb, DMA)
#define STEP_B(kst, kb, vst, vb, DMA) P2_STEP(sB, sA, pB0, pB1, pA0, pA1, kB0, kB1, kA0, kA1, vB0, vB1, vA0, vA1, kst, kb, vst, vb, DMA)
  constexpr int D = 4;
  const int tm = (xcd >> 1) * 4 + (i & 3), tn = (xcd & 1) * 8 + (i >> 2);
  const unsigned ab = WS_A + (unsigned)(tm * 4) * KS_A * 1024, wb = WS_W + (unsigned)tn * KS_A * 2048;
  bf16x8 a[D][4], wf[D][2];
  auto load = [&](int slot, int s) {
    const unsigned ks = (unsigned)((s < 28 ? KS_O + 8 * s : 8 * (s - 28)) + wid);
#pragma unroll
    for (int u = 0; u < 4; ++u) a[slot][u] = __builtin_bit_cast(bf16x8, __builtin_amdgcn_raw_buffer_load_b128(rs, l16, ab + (u * KS_A + ks) * 1024, SC1));
#pragma unroll
    for (int b = 0; b < 2; ++b) wf[slot][b] = __builtin_bit_cast(bf16x8, __builtin_amdgcn_raw_buffer_load_b128(rs, l16, wb + ks * 2048 + b * 1024, SC1));
  };
  gu64* const pkv = (gu64*)(ws + WS_FKV) + hd * 32 + (lane & 31);
  gu64* const pp1 = (gu64*)(ws + WS_FP1) + (lane < 32 ? r : (tm >> 1)) * 32 + (lane & 31);
  gu64* const pfw = (gu64*)(ws + WS_FW) + lane;
  u64 fkv = 0, f0 = 0, f1 = 0, f2 = 0, f3 = 0, f4 = 0, g2 = 0; unsigned gqu = 0;
  auto side = [&](int n) {
    if (n < 10) {
      const int u = n / 5, b = n % 5; const bool ok = wid >= 4 || b >= 3;
      const unsigned off = ok ? WS_A + (unsigned)((r * 8 + (wid & 3) * 2 + u) * KS_A + KS_O + hd * 7 + (wid >= 4 ? b + 2 : b - 3)) * 1024 : 0xfffff000u;
      st16(rs, l16, off, hw[n]);
    }
    if (n >= 6 && n < 11) {
      const int m = n - 6, fl = wid + 8 * m < 36 ? wid + 8 * m : wid, f = 36 * w + fl, ks = f >> 5, fb = f & 31;
      const char* src = (ks < KS_O ? wo + (size_t)ks * 16 * 2048 : wd + (size_t)(ks - KS_O) * 16 * 2048) + fb * 64;
      glds16(src, (unsigned)((lane >> 2) * 2048 + (lane & 3) * 16), ldsb + AT_WT + (wid + 8 * m) * 1024);
    }
    if (n >= 16 && n < 21) {
      const int m = n - 16, f = 36 * w + wid + 8 * m, ks = f >> 5, fb = f & 31;
      const int hi = lane >> 5, nat = ks >= KS_O;
      LAS const char* tp = lds + AT_WT + ((nat ? 8 : 4) * hi + ((lane & 15) >> 2)) * 64 + 32 * ((lane >> 4) & 1) + 8 * (lane & 3) + (wid + 8 * m) * 1024;
      const bf16x8 v = tr_frag(tp, tp + (nat ? 4 : 8) * 64);
      st16(rs, l16, wid + 8 * m < 36 ? WS_W + (unsigned)(((fb >> 1) * KS_A + ks) * 2 + (fb & 1)) * 1024 : 0xfffff000u, __builtin_bit_cast(u32x4, v));
    }
    if (n >= 54 && n < 62) {
      const int s = (n - 54) >> 1; const unsigned ks = (unsigned)(KS_O + 8 * s + wid);
      if ((n & 1) == 0) {
#pragma unroll
        for (int u = 0; u < 3; ++u) a[s][u] = __builtin_bit_cast(bf16x8, __builtin_amdgcn_raw_buffer_load_b128(rs, l16, ab + (u * KS_A + ks) * 1024, SC1));
      } else {
        a[s][3] = __builtin_bit_cast(bf16x8, __builtin_amdgcn_raw_buffer_load_b128(rs, l16, ab + (3 * KS_A + ks) * 1024, SC1));
#pragma unroll
        for (int b = 0; b < 2; ++b) wf[s][b] = __builtin_bit_cast(bf16x8, __builtin_amdgcn_raw_buffer_load_b128(rs, l16, wb + ks * 2048 + b * 1024, SC1));
      }
    }
  };
#pragma unroll
  for (int t = 0; t < NTILE; ++t) {
    const unsigned st = (t % 3) * STAGE, sn = ((t + 1) % 3) * STAGE, sp = ((t + 2) % 3) * STAGE;
    STEP_A(st, 2, st, 0, side(8 * t); if (t >= 2 && t + 1 < NTILE) P2_DMA1(1, t + 1, sn)); STEP_B(st, 3, st, 1, side(8 * t + 1); if (t >= 2 && t + 1 < NTILE) P2_DMA1(3, t + 1, sn));
    STEP_A(st, 4, st, 2, side(8 * t + 2)); STEP_B(st, 5, st, 3, side(8 * t + 3)); STEP_A(st, 6, st, 4, side(8 * t + 4)); STEP_B(st, 7, st, 5, side(8 * t + 5));
    if (t == 0) { asm volatile("s_waitcnt vmcnt(10)\n\ts_barrier" ::: "memory");
      fkv = __hip_atomic_load(pkv, RLX_AGENT); SBAR(); }
    else if (t == 1) { WAIT_VM(0); flag_poll(fkv, pkv, epoch); asm volatile("s_barrier" ::: "memory"); }
    else if (t < 7) {
      WAIT_VM(0);
      if (t == 6) { flag_poll(f0, pp1, epoch); flag_poll(f1, pfw, epoch); flag_poll(f2, pfw + 64, epoch); flag_poll(f3, pfw + 128, epoch); flag_poll(f4, pfw + 192, epoch); }
      asm volatile("s_barrier" ::: "memory");
    }
    if (t == 1 && threadIdx.x == 0) flag_set(ws, WS_FP1 + (unsigned)(r * 32 + hd) * 8, epoch);
    if (t == 2 && threadIdx.x == 0) flag_set(ws, WS_FW + (unsigned)w * 8, epoch);
    if (t == 5) { f0 = __hip_atomic_load(pp1, RLX_AGENT); f1 = __hip_atomic_load(pfw, RLX_AGENT); f2 = __hip_atomic_load(pfw + 64, RLX_AGENT); f3 = __hip_atomic_load(pfw + 128, RLX_AGENT); f4 = __hip_atomic_load(pfw + 192, RLX_AGENT); SBAR(); }
    if (t == 6) {
      gqu = __hip_atomic_load((GAS unsigned*)(ws + WS_G + (T * 32 + (lane & 31)) * 4), RLX_AGENT);
      g2 = __hip_atomic_load((gu64*)(ws + WS_G + (tm * 128 + lane * 2) * 4), RLX_AGENT);
      SBAR();
    }
    STEP_A(sn, 0, st, 6, side(8 * t + 6); if (t >= 1 && t + 2 < NTILE) P2_DMA1(0, t + 2, sp)); STEP_B(sn, 1, st, 7, side(8 * t + 7); if (t >= 1 && t + 2 < NTILE) P2_DMA1(2, t + 2, sp));
  }
  o = MFMA32(vB0, BF(pB0), o); o = MFMA32(vB1, BF(pB1), o);
  ls0 = MFMA16(ones, BF(pB0), ls0); ls1 = MFMA16(ones, BF(pB1), ls1);
  STAMP_AT(2);
  const float gq = __uint_as_float(gqu);

  float l = (lane & 16) ? ls0[1] + ls1[1] : ls0[0] + ls1[0];
  float amax = 0.f;
#pragma unroll
  for (int e = 0; e < 16; ++e) amax = fmaxf(amax, fabsf(o[e]));
  const bool bad = !(l < 1.0995116e12f) || !(amax < INFINITY);
  if (__builtin_expect(__any(bad), 0)) {
    float m = -INFINITY; l = 0.f;
#pragma unroll
    for (int e = 0; e < 16; ++e) o[e] = 0.f;
    for (int b = 0; b < NT32; ++b) {
      const unsigned ko = WS_K + (unsigned)(hd * NT32 + b) * 2048, vo = WS_V + (unsigned)(hd * NT32 + b) * 2048;
      const bf16x8 k0 = BF(__builtin_amdgcn_raw_buffer_load_b128(rs, l16, ko, SC1)), k1 = BF(__builtin_amdgcn_raw_buffer_load_b128(rs, l16, ko + 1024, SC1));
      const bf16x8 v0 = BF(__builtin_amdgcn_raw_buffer_load_b128(rs, l16, vo, SC1)), v1 = BF(__builtin_amdgcn_raw_buffer_load_b128(rs, l16, vo + 1024, SC1));
      f32x16 s = {}; s = MFMA32(k0, q0, s); s = MFMA32(k1, q1, s);
      float mb = s[0];
#pragma unroll
      for (int e = 1; e < 16; ++e) mb = fmaxf(mb, s[e]);
      const float mn = fmaxf(m, halfmax(mb)); const float al = __builtin_amdgcn_exp2f(m - mn); m = mn;
#pragma unroll
      for (int e = 0; e < 16; ++e) { s[e] = __builtin_amdgcn_exp2f(s[e] - mn); o[e] *= al; }
      const u32x4 w0 = pack8(s, 0, 1.f), w1 = pack8(s, 8, 1.f);
      float rsum = 0.f;
#pragma unroll
      for (int e = 0; e < 4; ++e) rsum += __uint_as_float(w0[e] << 16) + __uint_as_float(w0[e] & 0xffff0000u) + __uint_as_float(w1[e] << 16) + __uint_as_float(w1[e] & 0xffff0000u);
      { auto rr = __builtin_amdgcn_permlane32_swap(__float_as_uint(rsum), __float_as_uint(rsum), false, false); rsum = __uint_as_float(rr[0]) + __uint_as_float(rr[1]); }
      l = l * al + rsum;
      o = MFMA32(v0, BF(w0), o); o = MFMA32(v1, BF(w1), o);
    }
  }
  const float sc = gq / l;
  const unsigned oo = WS_A + (unsigned)(T * KS_A + hd * 2) * 1024;
  st16(rs, l16, oo, pack8(o, 0, sc)); st16(rs, l16, oo + 1024, pack8(o, 8, sc));
  LAS float* gl = (LAS float*)(lds + P3_G + wid * 512);
  *(LAS f32x2*)(gl + lane * 2) = (f32x2){1.f - __uint_as_float((unsigned)g2), 1.f - __uint_as_float((unsigned)(g2 >> 32))};
  WAITVM_BAR(0);
  if (threadIdx.x == 0) flag_set(ws, WS_FO + (unsigned)(r * 32 + hd) * 8, epoch);
  STAMP_AT(3); STAMP_END();

  STAMP_NEXT(); STAMP_AT(0);
  const int l31 = lane & 31;
  f32x16 acc[4][2];
#pragma unroll
  for (int u = 0; u < 4; ++u)
#pragma unroll
    for (int b = 0; b < 2; ++b)
#pragma unroll
      for (int e = 0; e < 16; ++e) acc[u][b][e] = 0.f;
  auto mma = [&](int d) {
#pragma unroll
    for (int u = 0; u < 4; ++u)
#pragma unroll
      for (int b = 0; b < 2; ++b) acc[u][b] = MFMA32(wf[d][b], a[d][u], acc[u][b]); };
#pragma unroll 1
  for (int so = 0; so < 28 - D; so += D) {
#pragma unroll
    for (int d = 0; d < D; ++d) { mma(d); SBAR(); load(d, so + d + D); SBAR(); }
  }
  flags_wait((gu64*)(ws + WS_FO) + (tm >> 1) * 32 + (lane & 31), epoch);
#pragma unroll
  for (int d = 0; d < D; ++d) { mma(d); SBAR(); load(d, 28 + d); SBAR(); }
#pragma unroll
  for (int u = 0; u < 4; ++u) { const float gt = gl[u * 32 + l31];
#pragma unroll
    for (int b = 0; b < 2; ++b)
#pragma unroll
      for (int e = 0; e < 16; ++e) acc[u][b][e] *= gt;
    SBAR(); }
#pragma unroll
  for (int d = 0; d < D; ++d) { mma(d); SBAR(); load(d, 32 + d); SBAR(); }
#pragma unroll
  for (int d = 0; d < D; ++d) mma(d);
  STAMP_AT(1);
  int tz = threadIdx.x; asm volatile("" : "+v"(tz));
  const int ln = tz & 63;
  LAS f32x4* red = (LAS f32x4*)lds + ln;
  f32x4 res[4];
#pragma unroll
  for (int ph = 0; ph < 2; ++ph) {
    if (ph) __syncthreads();
#pragma unroll
    for (int u = 0; u < 4; ++u)
#pragma unroll
      for (int b = 0; b < 2; ++b)
#pragma unroll
        for (int e = 0; e < 2; ++e) { const int oi = 8 * ph + 4 * e;
          red[((wid * 8 + u * 2 + b) * 2 + e) * 64] = (f32x4){acc[u][b][oi], acc[u][b][oi + 1], acc[u][b][oi + 2], acc[u][b][oi + 3]}; }
    __syncthreads();
    if (ph == 0) STAMP_AT(2);
#pragma unroll
    for (int e = 0; e < 2; ++e) { f32x4 s = red[((0 * 8 + wid) * 2 + e) * 64];
#pragma unroll
      for (int g = 1; g < 8; ++g) s += red[((g * 8 + wid) * 2 + e) * 64];
      res[2 * ph + e] = s; }
  }
  {
    unsigned pk[4][2];
#pragma unroll
    for (int e = 0; e < 4; ++e) { pk[e][0] = cvtpk(res[e][0], res[e][1]); pk[e][1] = cvtpk(res[e][2], res[e][3]); }
    const __amdgpu_buffer_rsrc_t ro = __builtin_amdgcn_make_buffer_rsrc(out, 0, SEQ * DM * 2, 0x00020000);
    const unsigned ob = (unsigned)((tm * 128 + (wid >> 1) * 32 + (ln & 31)) * 2048 + (tn * 64 + (wid & 1) * 32) * 2 + (ln >> 5) * 16);
#pragma unroll
    for (int e = 0; e < 4; e += 2) {
      const auto r0 = __builtin_amdgcn_permlane32_swap(pk[e][0], pk[e + 1][0], false, false), r1 = __builtin_amdgcn_permlane32_swap(pk[e][1], pk[e + 1][1], false, false);
      const u32x4 v = {r0[0], r1[0], r0[1], r1[1]};
      __builtin_amdgcn_raw_buffer_store_b128(v, ro, ob + e * 16, 0, OUT_AUX);
    }
  }
  STAMP_AT(3); STAMP_END();
}

__global__ __launch_bounds__(512, 2) void mega(const char* x, const char* wqkv, const char* wo, const char* wg, const char* wu, const char* wd, const char* gatew, char* ws, char* out) {
  extern __shared__ __attribute__((aligned(16))) char smem[];
  LAS char* lds = (LAS char*)smem;
  const int tid = threadIdx.x, lane = tid & 63, wid = __builtin_amdgcn_readfirstlane(tid >> 6);
  const int w = blockIdx.x, xcd = w & 7, i = w >> 3, j = xcd * 4 + (i & 3), r = i >> 2;
  u64 epoch; u32x4 hw[10];
  if (wid < 4) epoch = phase1<0>(hw, lds, x, wqkv, wo, wg, wu, wd, gatew, ws, w, j, r, wid, lane); else epoch = phase1<1>(hw, lds, x, wqkv, wo, wg, wu, wd, gatew, ws, w, j, r, wid, lane);
  phase23(hw, lds, wo, wd, ws, out, epoch, w, j, r, xcd, i, wid, lane);
  if (w == 0 && tid == 0) flag_set(ws, WS_E, epoch);
}

extern "C" void kernel_launch(void* const* d_in, const int* in_sizes, int n_in, void* d_out, int out_size, void* d_ws, size_t ws_size, hipStream_t stream) {
  (void)hipFuncSetAttribute((const void*)mega, hipFuncAttributeMaxDynamicSharedMemorySize, lm::BYTES);
  mega<<<256, 512, lm::BYTES, stream>>>((const char*)d_in[0], (const char*)d_in[1], (const char*)d_in[2], (const char*)d_in[4], (const char*)d_in[5], (const char*)d_in[6],
                                        (const char*)d_in[7], (char*)d_ws, (char*)d_out);
}
```

```cpp
#include <hip/hip_runtime.h>
#include <stdint.h>

typedef __bf16 bf16x8 __attribute__((ext_vector_type(8)));
typedef __bf16 bf16x2 __attribute__((ext_vector_type(2)));
typedef short s16x4 __attribute__((ext_vector_type(4)));
typedef float f32x16 __attribute__((ext_vector_type(16)));
typedef float f32x4 __attribute__((ext_vector_type(4)));
typedef float f32x2 __attribute__((ext_vector_type(2)));
typedef unsigned u32x4 __attribute__((ext_vector_type(4)));
typedef unsigned u32x2 __attribute__((ext_vector_type(2)));
typedef unsigned long long u64;
#define LAS __attribute__((address_space(3)))
#define GAS __attribute__((address_space(1)))
#define DI __device__ __forceinline__
#define SBAR() __builtin_amdgcn_sched_barrier(0)
#define PIN(x) asm volatile("" : "+v"(x))
#define MFMA32(a, b, c) __builtin_amdgcn_mfma_f32_32x32x16_bf16((a), (b), (c), 0, 0, 0)
#define MFMA16(a, b, c) __builtin_amdgcn_mfma_f32_16x16x32_bf16((a), (b), (c), 0, 0, 0)
#define RLX_AGENT __ATOMIC_RELAXED, __HIP_MEMORY_SCOPE_AGENT
#define SC1 16
#define OUT_AUX 0

constexpr int SEQ = 2048, DM = 1024, FF = 3584, NH = 32;
constexpr int NT32 = SEQ / 32;
constexpr int KS_O = DM / 16;
constexpr int KS_H = FF / 16;
constexpr int KS_A = KS_O + KS_H;
constexpr unsigned WS_E = 0;
constexpr unsigned WS_FKV = 256;
constexpr unsigned WS_FP1 = WS_FKV + 32 * 32 * 8;
constexpr unsigned WS_FO = WS_FP1 + 256 * 8;
constexpr unsigned WS_G = 16384;
constexpr unsigned WS_K = 32768;
constexpr unsigned WS_V = WS_K + NH * NT32 * 2048;
constexpr unsigned WS_A = WS_V + NH * NT32 * 2048;
constexpr unsigned WS_END = WS_A + NT32 * KS_A * 1024;
static_assert(WS_FO + 256 * 8 <= WS_G, "flags");

DI unsigned cvtpk(float lo, float hi) { f32x2 v = {lo, hi}; bf16x2 b = __builtin_convertvector(v, bf16x2); return __builtin_bit_cast(unsigned, b); }
DI u32x4 pack8(const f32x16& a, int o, float s) {
  u32x4 w; w[0] = cvtpk(a[o + 0] * s, a[o + 1] * s); w[1] = cvtpk(a[o + 2] * s, a[o + 3] * s); w[2] = cvtpk(a[o + 4] * s, a[o + 5] * s); w[3] = cvtpk(a[o + 6] * s, a[o + 7] * s); return w; }
DI bf16x8 tr_frag(LAS const char* p0, LAS const char* p1) {
  s16x4 lo = __builtin_amdgcn_ds_read_tr16_b64_v4i16((LAS s16x4*)p0);
  s16x4 hi = __builtin_amdgcn_ds_read_tr16_b64_v4i16((LAS s16x4*)p1);
  return __builtin_bit_cast(bf16x8, __builtin_shufflevector(lo, hi, 0, 1, 2, 3, 4, 5, 6, 7)); }
DI void glds16(const char* sbase, unsigned voff, unsigned ldst) {
  asm volatile("s_mov_b32 m0, %0\n\ts_nop 0\n\tglobal_load_lds_dwordx4 %1, %2" ::"s"(ldst), "v"(voff), "s"(sbase) : "memory"); }
DI void glds16_sc1(const char* sbase, unsigned voff, unsigned ldst) {
  asm volatile("s_mov_b32 m0, %0\n\ts_nop 0\n\tglobal_load_lds_dwordx4 %1, %2 sc1" ::"s"(ldst), "v"(voff), "s"(sbase) : "memory"); }
DI void glds16c(int on, const char* sbase, unsigned voff, unsigned ldst) {
  asm volatile("s_cmp_lt_i32 %3, 1\n\ts_cbranch_scc1 1f\n\ts_mov_b32 m0, %0\n\ts_nop 0\n\tglobal_load_lds_dwordx4 %1, %2\n1:" ::"s"(ldst), "v"(voff), "s"(sbase), "s"(on) : "memory", "scc"); }
DI void l2_prefetch(const char* sbase, unsigned voff, unsigned ldst) {
  asm volatile("s_mov_b32 m0, %0\n\ts_nop 0\n\tglobal_load_lds_dword %1, %2 sc1" ::"s"(ldst), "v"(voff), "s"(sbase) : "memory"); }
#define STAMP_DECL2(kid)
#define STAMP_NEXT()
#define STAMP_DECL(kid)
#define STAMP_AT(i)
#define STAMP_END()
#define WAIT_VM(n) asm volatile("s_waitcnt vmcnt(" #n ")" ::: "memory")
#define WAITVM_BAR(n) asm volatile("s_waitcnt vmcnt(" #n ") lgkmcnt(0)\n\ts_barrier" ::: "memory")

typedef GAS u64 gu64;
DI void flag_set(char* ws, unsigned off, u64 epoch) { __hip_atomic_store((gu64*)(ws + off), epoch, RLX_AGENT); }
DI void flag_poll(u64 v, const gu64* p, u64 epoch) {
  unsigned n;
  asm volatile("s_mov_b32 %1, 0\n1:\n\tv_cmp_ne_u64 vcc, %2, %0\n\ts_cbranch_vccz 2f\n\ts_add_u32 %1, %1, 1\n\ts_cmp_gt_u32 %1, 0xffff\n\ts_cbranch_scc1 2f\n\ts_sleep 1\n\t"
               "global_load_dwordx2 %0, %3, off sc1\n\ts_waitcnt vmcnt(0)\n\ts_branch 1b\n2:"
               : "+v"(v), "=&s"(n) : "s"(epoch), "v"(p) : "vcc", "scc", "memory");
}
DI void flags_wait(const gu64* p, u64 epoch) { flag_poll(__hip_atomic_load(p, RLX_AGENT), p, epoch); }
DI void st16(__amdgpu_buffer_rsrc_t rs, unsigned voff, unsigned soff, u32x4 v) { __builtin_amdgcn_raw_buffer_store_b128(v, rs, voff + soff, 0, SC1); }

namespace lm {
constexpr int P1_X = 0, P1_QA = 32768, P1_QB = 38912, P1_WG = 45056, P1_WU = 59392, P1_STAGE = 73728, P1_PF = 2 * P1_STAGE;
constexpr int AT_STAGE = 32768, GW = 3 * AT_STAGE, GW_D = 5, GL = GW + 4 * GW_D * 2048;
constexpr int SYNC = 163328, BYTES = 163840;
}

constexpr float QSCALE = 0.17677669529663687f * 1.4426950408889634f;
template <int WC> DI u64 p1_loop(f32x4 (&acc)[4][10], LAS char* lds, const char* x, const char* wqkv, const char* wo, const char* wg, const char* wu, const char* wd, const char* gatew, char* ws, int w, int j, int r, int wid, int lane) {
  using namespace lm;
  STAMP_DECL(0); STAMP_AT(0);
  const int wr = wid & 3, g4 = lane >> 4, c16 = lane & 15;
  const unsigned ldsb = (unsigned)(uintptr_t)lds;
  const char* xsrc = x + (size_t)(r * 256 + wid * 32) * 2048;
  unsigned xoff[2];
#pragma unroll
  for (int e = 0; e < 2; ++e) xoff[e] = (unsigned)((lane >> 3) * 2048 + (((lane & 7) ^ ((4 * e + (lane >> 4)) & 7)) << 4));
  const char* wsrc[5]; unsigned woff[5], wdst[5], wstep[5];
#pragma unroll
  for (int m = 0; m < 5; ++m) {
    const int p = wid + 8 * m; const bool isq = p < 12, isa = p < 6, isg = p < 26;
    const int pp = isq ? (isa ? p : p - 6) : (isg ? p - 12 : p - 26);
    wsrc[m] = isq ? wqkv : (isg ? wg : wu);
    wdst[m] = (unsigned)__builtin_amdgcn_readfirstlane((isq ? (isa ? P1_QA : P1_QB) : (isg ? P1_WG : P1_WU)) + pp * 1024);
    wstep[m] = isq ? 64 * 6144 : 64 * 7168;
    const int cc = 64 * pp + lane, rq = cc / 6, pq = cc % 6, rg = cc / 14, pg = cc % 14;
    const int colq = isa ? (pq < 4 ? pq * 8 : 1024 + (pq - 4) * 8) : (pq < 2 ? 1024 + (2 + pq) * 8 : 2048 + (pq - 2) * 8);
    woff[m] = isq ? (unsigned)((rq ^ ((rq >> 1) & 4)) * 6144 + (colq + j * 32) * 2) : (unsigned)((rg ^ ((rg >> 1) & 4)) * 7168 + (j * 112 + pg * 8) * 2);
  }
#define GLC glds16c
#define P1_DMA_X(on, q, t, st) GLC(on, xsrc + (size_t)(t) * 128 + (q) * 8 * 2048, xoff[(q) & 1], ldsb + (st) + P1_X + (wid * 4 + (q)) * 1024)
#define P1_DMA_W(on, m, t, st) GLC(on, wsrc[m] + (size_t)(t) * wstep[m], woff[m], ldsb + (st) + wdst[m])
  STAMP_AT(6);
#pragma unroll
  for (int q = 0; q < 4; ++q) P1_DMA_X(1, q, 0, 0);
#pragma unroll
  for (int m = 0; m < 5; ++m) P1_DMA_W(1, m, 0, 0);
  STAMP_AT(7);
  asm volatile("s_barrier" ::: "memory");
  STAMP_AT(8);
#pragma unroll
  for (int q = 0; q < 4; ++q) P1_DMA_X(1, q, 1, P1_STAGE);
#pragma unroll
  for (int m = 0; m < 5; ++m) P1_DMA_W(1, m, 1, P1_STAGE);
  unsigned pfoff, pfstep;
  {
    const int jj = j & 3, xg = j >> 2, row = (r * 4 + jj) * 2 + (wid & 1), l6 = lane % 6, l7 = (lane & 31) % 7;
    const unsigned ox = (unsigned)((r * 256 + jj * 64 + wid * 16 + (lane & 15)) * 2048);
    const unsigned oq = (unsigned)(row * 6144 + (l6 >> 1) * 2048 + xg * 256 + (l6 & 1) * 128), og = (unsigned)(row * 7168 + xg * 896 + l7 * 128);
    pfoff = wid < 4 ? ox : (wid < 6 ? oq : og); pfstep = wid < 4 ? 128 : (wid < 6 ? 64 * 6144 : 64 * 7168);
  }
  const char* pfl = (wid >= 6 ? (lane < 32 ? wg : wu) : (wid < 4 ? x : wqkv)) + pfoff;
  const bool pfw = wid == 0 && lane >= 16 && lane < 34;
  if (pfw) { const int f = 36 * w + 2 * (lane - 16), ks = f >> 5, fb = f & 31;
    pfl = (ks < KS_O ? wo + (size_t)ks * 16 * 2048 : wd + (size_t)(ks - KS_O) * 16 * 2048) + fb * 64; pfstep = 2048; }
  auto pf = [&](int t, int tw) { asm volatile("s_mov_b32 m0, %0\n\ts_nop 0\n\tglobal_load_lds_dword %1, off sc1" ::"s"(ldsb + P1_PF), "v"(pfl + (size_t)(unsigned)((pfw ? tw : t) * pfstep)) : "memory"); };
  unsigned xa[2];
#pragma unroll
  for (int ks = 0; ks < 2; ++ks) xa[ks] = (unsigned)(P1_X + (wr * 64 + c16) * 128 + (((4 * ks + g4) ^ ((c16 >> 1) & 7)) << 4));
  unsigned qa[2], ga[2];
#pragma unroll
  for (int t = 0; t < 2; ++t) { const int prow = 8 * g4 + 4 * (t ^ (g4 & 1)) + (c16 >> 2);
    qa[t] = (unsigned)(P1_QA + prow * 96 + 8 * (c16 & 3)); ga[t] = (unsigned)(P1_WG + prow * 224 + 8 * (c16 & 3)); }
#pragma unroll
  for (int u = 0; u < 4; ++u)
#pragma unroll
    for (int b = 0; b < 10; ++b) acc[u][b] = (f32x4){0.f, 0.f, 0.f, 0.f};
  bf16x8 xf[2][4]; s16x4 wlo[4], whi[4];
  auto ldx = [&](int u, unsigned st, int ks) { xf[ks][u] = *(LAS const bf16x8*)(lds + st + xa[ks] + u * 2048); };
  auto ldw = [&](int b, unsigned st, int ks, int h) {
    const int slot = (10 * ks + b) & 3;
    unsigned a;
    if (WC == 0 && b < 6) a = qa[h] + ks * 32 * 96 + (b < 3 ? b * 32 : (P1_QB - P1_QA) + (b - 3) * 32);
    else { const int gb = WC == 0 ? (b - 6) >> 1 : 2 + (b >> 1); a = ga[h] + ks * 32 * 224 + gb * 32 + ((b & 1) ? P1_WU - P1_WG : 0); }
    const s16x4 v = __builtin_amdgcn_ds_read_tr16_b64_v4i16((LAS s16x4*)(lds + st + a));
    if (h == 0) wlo[slot] = v; else whi[slot] = v; };
  auto mma1 = [&](int b, int ks, int u) {
    const int slot = (10 * ks + b) & 3;
    const bf16x8 wf = __builtin_bit_cast(bf16x8, __builtin_shufflevector(wlo[slot], whi[slot], 0, 1, 2, 3, 4, 5, 6, 7));
    if (WC == 0 && (b == 4 || b == 5)) acc[u][b] = MFMA16(xf[ks][u], wf, acc[u][b]); else acc[u][b] = MFMA16(wf, xf[ks][u], acc[u][b]); };
#define NOP (void)0
  pf(2, 0); pf(3, 0);
  const u64 e0 = __hip_atomic_load((gu64*)(ws + WS_E), RLX_AGENT);
  l2_prefetch(gatew, (unsigned)(lane & 15) * 128, ldsb + P1_PF);
  STAMP_AT(9);
  asm volatile("s_waitcnt vmcnt(13)\n\ts_barrier" ::: "memory");
  STAMP_AT(1);
#pragma unroll
  for (int u = 0; u < 4; ++u) ldx(u, 0, 0);
#pragma unroll
  for (int b = 0; b < 3; ++b) { ldw(b, 0, 0, 0); ldw(b, 0, 0, 1); }
  unsigned cur = 0;
#pragma unroll 1
  for (int t = 0; t < 16; ++t) {
    const unsigned nxt = P1_STAGE - cur;
    const int m1 = t * (15 - t), m2 = 14 - t;
    const int tp = t + 4 < 16 ? t + 4 : 15;
#define P1_GRP(b, ks, R0, R1, R2, R3) do { mma1(b, ks, 0); SBAR(); R0; SBAR(); mma1(b, ks, 1); SBAR(); R1; SBAR(); mma1(b, ks, 2); SBAR(); R2; SBAR(); mma1(b, ks, 3); SBAR(); R3; SBAR(); } while (0)
#define LDW_AHEAD0(b, h) do { if ((b) + 3 < 10) ldw((b) + 3, cur, 0, h); else ldw((b) + 3 - 10, cur, 1, h); } while (0)
#define P1_G0(b, SIDE) P1_GRP(b, 0, LDW_AHEAD0(b, 0), LDW_AHEAD0(b, 1), if ((b) < 4) ldx(b, cur, 1), SIDE)
    P1_G0(0, P1_DMA_X(m1, 3, t + 1, nxt)); P1_G0(1, P1_DMA_W(m1, 0, t + 1, nxt)); P1_G0(2, P1_DMA_W(m1, 1, t + 1, nxt)); P1_G0(3, P1_DMA_W(m1, 2, t + 1, nxt));
    P1_G0(4, P1_DMA_W(m1, 3, t + 1, nxt)); P1_G0(5, P1_DMA_W(m1, 4, t + 1, nxt)); P1_G0(6, pf(tp, t)); P1_G0(7, NOP); P1_G0(8, NOP); P1_G0(9, NOP);
#define P1_G1(b) P1_GRP(b, 1, ldw((b) + 3, cur, 1, 0), ldw((b) + 3, cur, 1, 1), NOP, NOP)
    P1_G1(0); P1_G1(1); P1_G1(2); P1_G1(3); P1_G1(4); P1_G1(5); P1_G1(6);
    asm volatile("s_cmp_lg_u32 %0, 0\n\ts_cbranch_scc1 1f\n\ts_waitcnt vmcnt(5) lgkmcnt(0)\n\ts_branch 2f\n1:\n\ts_waitcnt vmcnt(1) lgkmcnt(0)\n2:\n\ts_barrier" ::"s"(t) : "memory", "scc");
    P1_GRP(7, 1, ldx(0, nxt, 0); ldw(0, nxt, 0, 0), ldx(1, nxt, 0); ldw(0, nxt, 0, 1), ldx(2, nxt, 0), ldx(3, nxt, 0); P1_DMA_X(m2, 0, t + 2, cur));
    P1_GRP(8, 1, ldw(1, nxt, 0, 0), ldw(1, nxt, 0, 1), NOP, P1_DMA_X(m2, 1, t + 2, cur));
    P1_GRP(9, 1, ldw(2, nxt, 0, 0), ldw(2, nxt, 0, 1), NOP, P1_DMA_X(m2, 2, t + 2, cur));
    cur = nxt;
  }
  WAITVM_BAR(0);
  STAMP_AT(2);
  return (((u64)(unsigned)__builtin_amdgcn_readfirstlane((unsigned)(e0 >> 32)) << 32) | (unsigned)__builtin_amdgcn_readfirstlane((unsigned)e0)) + 1;
}

DI u32x4 frag16(const f32x4& lo, const f32x4& hi, float s) {
  const auto r0 = __builtin_amdgcn_permlane16_swap(cvtpk(lo[0] * s, lo[1] * s), cvtpk(hi[0] * s, hi[1] * s), false, false);
  const auto r1 = __builtin_amdgcn_permlane16_swap(cvtpk(lo[2] * s, lo[3] * s), cvtpk(hi[2] * s, hi[3] * s), false, false);
  return (u32x4){r0[0], r1[0], r0[1], r1[1]}; }
DI u32x4 swiglu_frag(const f32x4& g0, const f32x4& u0, const f32x4& g1, const f32x4& u1) {
  f32x4 h0, h1;
#pragma unroll
  for (int i = 0; i < 4; ++i) { h0[i] = g0[i] * __builtin_amdgcn_rcpf(1.f + __builtin_amdgcn_exp2f(-1.4426950408889634f * g0[i])) * u0[i];
                                h1[i] = g1[i] * __builtin_amdgcn_rcpf(1.f + __builtin_amdgcn_exp2f(-1.4426950408889634f * g1[i])) * u1[i]; }
  return frag16(h0, h1, 1.f); }
DI void gate_store(const u32x4& a0, const u32x4& a1, const u32x4& b0, const u32x4& b1, char* ws, int tok) {
  float s = 0.f;
#pragma unroll
  for (int e = 0; e < 4; ++e) { const unsigned ua0 = a0[e], ub0 = b0[e], ua1 = a1[e], ub1 = b1[e];
    s = __builtin_amdgcn_fdot2_f32_bf16(__builtin_bit_cast(bf16x2, ua0), __builtin_bit_cast(bf16x2, ub0), s, false);
    s = __builtin_amdgcn_fdot2_f32_bf16(__builtin_bit_cast(bf16x2, ua1), __builtin_bit_cast(bf16x2, ub1), s, false); }
#pragma unroll
  for (int o = 32; o >= 1; o >>= 1) s += __shfl_xor(s, o);
  const float g = __builtin_amdgcn_rcpf(1.f + __builtin_amdgcn_exp2f(-1.4426950408889634f * s));
  __hip_atomic_store((GAS unsigned*)(ws + WS_G + tok * 4), __float_as_uint(g), RLX_AGENT);
}
DI unsigned lds_arrive(unsigned addr, int lane) {
  unsigned one = lane == 0 ? 1u : 0u, old;
  asm volatile("s_waitcnt lgkmcnt(0)\n\tds_add_rtn_u32 %0, %1, %2\n\ts_waitcnt lgkmcnt(0)" : "=&v"(old) : "v"(addr), "v"(one) : "memory");
  return (unsigned)__builtin_amdgcn_readfirstlane(old);
}
DI void lds_meet(unsigned addr, int lane, unsigned target) {
  unsigned one = lane == 0 ? 1u : 0u, v, sv, n;
  asm volatile("s_waitcnt lgkmcnt(0)\n\tds_add_u32 %3, %4\n\ts_mov_b32 %2, 0\n1:\n\tds_read_b32 %0, %3\n\ts_waitcnt lgkmcnt(0)\n\tv_readfirstlane_b32 %1, %0\n\ts_add_u32 %2, %2, 1\n\t"
               "s_cmp_ge_u32 %1, %5\n\ts_cbranch_scc1 2f\n\ts_cmp_lt_u32 %2, 0x100000\n\ts_cbranch_scc1 1b\n2:"
               : "=&v"(v), "=&s"(sv), "=&s"(n) : "v"(addr), "v"(one), "s"(target) : "memory", "scc");
}
DI float halfmax(float m) { auto rr = __builtin_amdgcn_permlane32_swap(__float_as_uint(m), __float_as_uint(m), false, false); return fmaxf(__uint_as_float(rr[0]), __uint_as_float(rr[1])); }
#define BF(x) __builtin_bit_cast(bf16x8, x)

DI void path_attn(LAS char* lds, const char* x, const char* wqkv, const char* wo, const char* wg, const char* wu, const char* wd, const char* gatew, char* ws, int w, int hd, int r, int wid, int lane) {
  using namespace lm;
  constexpr int NTILE = 8, STAGE = AT_STAGE;
  f32x4 acc[4][10];
  const u64 epoch = p1_loop<0>(acc, lds, x, wqkv, wo, wg, wu, wd, gatew, ws, w, hd, r, wid, lane);
  STAMP_DECL2(0);
  const __amdgpu_buffer_rsrc_t rs = __builtin_amdgcn_make_buffer_rsrc(ws, 0, WS_END, 0x00020000);
  const unsigned ldsb = (unsigned)(uintptr_t)lds, l16 = lane * 16;
  const int g4 = lane >> 4, c16 = lane & 15;
  const int gtok = r * 256 + hd * 8 + wid;
  const u32x4* gxp = (const u32x4*)(x + (size_t)gtok * 2048) + lane * 2; const u32x4* ggp = (const u32x4*)gatew + lane * 2;
  const u32x4 ga0 = gxp[0], ga1 = gxp[1], gb0 = ggp[0], gb1 = ggp[1];
  SBAR();
  bf16x8 qf[2][2];
  {
    const unsigned vl16 = (unsigned)(((g4 & 1) * 32 + (g4 >> 1) * 16 + c16) * 16);
#pragma unroll
    for (int tb = 0; tb < 2; ++tb) {
      const int tl = wid * 2 + tb, T = r * 8 + tl;
      const unsigned go = (unsigned)(hd * NT32 + T) * 2048;
      LAS char* kl = lds + tl * 2048;
#pragma unroll
      for (int t = 0; t < 2; ++t) {
        const u32x4 kf = frag16(acc[2 * tb][2 + t], acc[2 * tb + 1][2 + t], 1.f);
        st16(rs, l16, WS_K + go + t * 1024, kf); *(LAS u32x4*)(kl + t * 1024 + l16) = kf;
        const f32x4 &v0 = acc[2 * tb + t][4], &v1 = acc[2 * tb + t][5];
        const auto s0 = __builtin_amdgcn_permlane32_swap(cvtpk(v0[0], v0[1]), cvtpk(v1[0], v1[1]), false, false);
        const auto s1 = __builtin_amdgcn_permlane32_swap(cvtpk(v0[2], v0[3]), cvtpk(v1[2], v1[3]), false, false);
        const u32x4 vf = {s0[0], s1[0], s0[1], s1[1]};
        st16(rs, vl16, WS_V + go + t * 1024, vf); *(LAS u32x4*)(kl + 16384 + t * 1024 + vl16) = vf;
        qf[tb][t] = BF(frag16(acc[2 * tb][t], acc[2 * tb + 1][t], QSCALE));
      }
    }
  }
  SBAR();
#pragma unroll
  for (int tb = 0; tb < 2; ++tb)
#pragma unroll
    for (int gb = 0; gb < 2; ++gb)
      st16(rs, l16, WS_A + (unsigned)((r * 8 + wid * 2 + tb) * KS_A + KS_O + hd * 7 + gb) * 1024,
           swiglu_frag(acc[2 * tb][6 + 2 * gb], acc[2 * tb][7 + 2 * gb], acc[2 * tb + 1][6 + 2 * gb], acc[2 * tb + 1][7 + 2 * gb]));
  gate_store(ga0, ga1, gb0, gb1, ws, gtok);
  SBAR();
  STAMP_AT(3);
  const char* kbase = ws + WS_K + (size_t)hd * NT32 * 2048; const char* vbase = ws + WS_V + (size_t)hd * NT32 * 2048;
#define P2_DMA1(e, t, st) glds16_sc1(((e) < 4 ? kbase : vbase) + (size_t)((r + (t)) & 7) * 16384 + wid * 4096 + ((e) & 3) * 1024, l16, ldsb + (st) + ((e) >> 2) * 16384 + wid * 4096 + ((e) & 3) * 1024)
  unsigned meet = 4;
  lds_meet(ldsb + SYNC, lane, meet);
  STAMP_AT(4); STAMP_END();
  STAMP_NEXT(); STAMP_AT(0);
  LAS const char* lp = lds + lane * 16;
  LAS const char* lp2 = lp + 2 * STAGE; asm volatile("" : "+v"(lp2));
#define LPS(st) ((st) >= 2 * STAGE ? lp2 - 2 * STAGE : lp)
#define KFR(st, b, h) (*(LAS const bf16x8*)(LPS(st) + (st) + (b) * 2048 + (h) * 1024))
#define VFR(st, b, h) (*(LAS const bf16x8*)(LPS(st) + (st) + 16384 + (b) * 2048 + (h) * 1024))
  f32x16 negm;
  {
    float m = -INFINITY;
#pragma unroll
    for (int b = 0; b < 4; ++b)
#pragma unroll
      for (int qb = 0; qb < 2; ++qb) {
        f32x16 s = {}; s = MFMA32(KFR(0, b, 0), qf[qb][0], s); s = MFMA32(KFR(0, b, 1), qf[qb][1], s);
#pragma unroll
        for (int e = 0; e < 16; ++e) m = fmaxf(m, s[e]);
      }
    m = halfmax(m);
#pragma unroll
    for (int e = 0; e < 16; ++e) negm[e] = -m;
    PIN(negm);
  }
  bf16x8 ones; { const bool on = ((lane & 1) == ((lane >> 4) & 1)); const __bf16 o = on ? (__bf16)1.0f : (__bf16)0.0f;
#pragma unroll
    for (int e = 0; e < 8; ++e) ones[e] = o; }
  PIN(ones);
  f32x16 o[2] = {}; f32x4 ls[2] = {}; f32x2 lv[2] = {};
  f32x16 sA[2], sB[2]; u32x4 pA[2][2], pB[2][2] = {}; bf16x8 kA[2], kB[2], vA[2], vB[2] = {};
#pragma unroll
  for (int qb = 0; qb < 2; ++qb) { sA[qb] = MFMA32(KFR(0, 0, 0), qf[qb][0], negm); sA[qb] = MFMA32(KFR(0, 0, 1), qf[qb][1], sA[qb]); }
  kB[0] = KFR(0, 1, 0); kB[1] = KFR(0, 1, 1);
  WAIT_VM(5);
  if (lane == 0) flag_set(ws, WS_FKV + (unsigned)((hd * 8 + r) * 4 + wid) * 8, epoch);
  STAMP_AT(1);
  auto start_job = [&](int n) {
    if (n == 0) { WAIT_VM(1);
      if (lds_arrive(ldsb + SYNC + 4, lane) == 7 && lane == 0) flag_set(ws, WS_FP1 + (unsigned)(r * 32 + hd) * 8, epoch); }
    if (n == 1) flags_wait((gu64*)(ws + WS_FKV) + (hd * 8 + ((r + 1 + ((lane >> 2) & 1)) & 7)) * 4 + (lane & 3), epoch);
    if (n >= 1 && n <= 4) {
#pragma unroll
      for (int e = 0; e < 4; ++e) P2_DMA1(((n - 1) & 1) * 4 + e, 1 + ((n - 1) >> 1), (1 + ((n - 1) >> 1)) * STAGE); }
  };
#define EX(S, i) S[i] = __builtin_amdgcn_exp2f(S[i])
#define PK(S, i) cvtpk(S[i], S[i + 1])
#define P2_HALF(q, SX, SY, PX, PY, KY, VY, R0, R1, R2, R3) do { \
    SY[q] = MFMA32(KY[0], qf[q][0], negm); SBAR(); R0; EX(SX[q], 0); EX(SX[q], 1); EX(SX[q], 2); EX(SX[q], 3); PIN(SX[q]); SBAR(); \
    o[q] = MFMA32(VY[0], BF(PY[q][0]), o[q]); SBAR(); R1; EX(SX[q], 4); EX(SX[q], 5); EX(SX[q], 6); PX[q][0][0] = PK(SX[q], 0); PX[q][0][1] = PK(SX[q], 2); PIN(SX[q]); PIN(PX[q][0]); SBAR(); \
    ls[q] = MFMA16(ones, BF(PY[q][0]), ls[q]); SBAR(); EX(SX[q], 7); SBAR(); PX[q][0][2] = PK(SX[q], 4); SBAR(); PX[q][0][3] = PK(SX[q], 6); PIN(PX[q][0]); SBAR(); \
    SY[q] = MFMA32(KY[1], qf[q][1], SY[q]); SBAR(); R2; EX(SX[q], 8); EX(SX[q], 9); EX(SX[q], 10); EX(SX[q], 11); PIN(SX[q]); SBAR(); \
    o[q] = MFMA32(VY[1], BF(PY[q][1]), o[q]); SBAR(); R3; EX(SX[q], 12); EX(SX[q], 13); EX(SX[q], 14); PX[q][1][0] = PK(SX[q], 8); PX[q][1][1] = PK(SX[q], 10); PIN(SX[q]); PIN(PX[q][1]); SBAR(); \
    ls[q] = MFMA16(ones, BF(PY[q][1]), ls[q]); SBAR(); EX(SX[q], 15); SBAR(); PX[q][1][2] = PK(SX[q], 12); SBAR(); PX[q][1][3] = PK(SX[q], 14); PIN(PX[q][1]); SBAR(); } while (0)
#define P2_STEP(SX, SY, PX, PY, KX, KY, VX, VY, kst, kb, vst, vb, DMA0, DMA1) do { \
    P2_HALF(0, SX, SY, PX, PY, KY, VY, VX[0] = VFR(vst, vb, 0), VX[1] = VFR(vst, vb, 1), NOP, DMA0); \
    P2_HALF(1, SX, SY, PX, PY, KY, VY, KX[0] = KFR(kst, kb, 0), KX[1] = KFR(kst, kb, 1), NOP, DMA1); } while (0)
#define STEP_A(kst, kb, vst, vb, DMA0, DMA1) P2_STEP(sA, sB, pA, pB, kA, kB, vA, vB, kst, kb, vst, vb, DMA0, DMA1)
#define STEP_B(kst, kb, vst, vb, DMA0, DMA1) P2_STEP(sB, sA, pB, pA, kB, kA, vB, vA, kst, kb, vst, vb, DMA0, DMA1)
  gu64* const pkv = (gu64*)(ws + WS_FKV) + hd * 32 + (lane & 31);
  gu64* const pp1 = (gu64*)(ws + WS_FP1) + r * 32 + (lane & 31);
  u64 fkv = 0, fp1 = 0; unsigned gqu[2] = {0, 0};
#pragma unroll
  for (int t = 0; t < NTILE; ++t) {
    const unsigned st = (t % 3) * STAGE, sn = ((t + 1) % 3) * STAGE, sp = ((t + 2) % 3) * STAGE;
    const bool d1 = t >= 2 && t + 1 < NTILE, d2 = t >= 1 && t + 2 < NTILE;
    STEP_A(st, 2, st, 0, if (d1) P2_DMA1(2, t + 1, sn), if (d1) P2_DMA1(3, t + 1, sn); if (t == 0) start_job(0));
    STEP_B(st, 3, st, 1, if (d1) P2_DMA1(6, t + 1, sn), if (d1) P2_DMA1(7, t + 1, sn); if (t == 0) start_job(1));
    STEP_A(st, 4, st, 2, NOP, if (t == 0) start_job(2)); STEP_B(st, 5, st, 3, NOP, if (t == 0) start_job(3)); STEP_A(st, 6, st, 4, NOP, if (t == 0) start_job(4)); STEP_B(st, 7, st, 5, NOP, NOP);
    if (t < 7) {
      if (t == 0) WAIT_VM(8);
      else WAIT_VM(0);
      if (t == 1) flag_poll(fkv, pkv, epoch);
      if (t == 6) flag_poll(fp1, pp1, epoch);
      meet += 4; lds_meet(ldsb + SYNC, lane, meet);
      if (t == 0) { fkv = __hip_atomic_load(pkv, RLX_AGENT); SBAR(); }
      if (t == 5) { fp1 = __hip_atomic_load(pp1, RLX_AGENT); SBAR(); }
      if (t == 6) {
#pragma unroll
        for (int qb = 0; qb < 2; ++qb) gqu[qb] = __hip_atomic_load((GAS unsigned*)(ws + WS_G + ((r * 8 + wid * 2 + qb) * 32 + (lane & 31)) * 4), RLX_AGENT);
        SBAR(); }
    }
    STEP_A(sn, 0, st, 6, if (d2) P2_DMA1(0, t + 2, sp), if (d2) P2_DMA1(1, t + 2, sp)); STEP_B(sn, 1, st, 7, if (d2) P2_DMA1(4, t + 2, sp), if (d2) P2_DMA1(5, t + 2, sp));
  }
#pragma unroll
  for (int qb = 0; qb < 2; ++qb) {
    o[qb] = MFMA32(vB[0], BF(pB[qb][0]), o[qb]); o[qb] = MFMA32(vB[1], BF(pB[qb][1]), o[qb]);
    ls[qb] = MFMA16(ones, BF(pB[qb][0]), ls[qb]); ls[qb] = MFMA16(ones, BF(pB[qb][1]), ls[qb]);
  }
  STAMP_AT(2);
#pragma unroll
  for (int qb = 0; qb < 2; ++qb) {
    float l = (lane & 16) ? ls[qb][1] : ls[qb][0];
    float amax = 0.f;
#pragma unroll
    for (int e = 0; e < 16; ++e) amax = fmaxf(amax, fabsf(o[qb][e]));
    const bool bad = !(l < 1.0995116e12f) || !(l > 9.094947e-13f) || !(amax < INFINITY);
    if (__builtin_expect(__any(bad), 0)) {
      float m = -INFINITY; l = 0.f;
      f32x16 oo = {};
      for (int b = 0; b < NT32; ++b) {
        const unsigned ko = WS_K + (unsigned)(hd * NT32 + b) * 2048, vo = WS_V + (unsigned)(hd * NT32 + b) * 2048;
        const bf16x8 k0 = BF(__builtin_amdgcn_raw_buffer_load_b128(rs, l16, ko, SC1)), k1 = BF(__builtin_amdgcn_raw_buffer_load_b128(rs, l16, ko + 1024, SC1));
        const bf16x8 v0 = BF(__builtin_amdgcn_raw_buffer_load_b128(rs, l16, vo, SC1)), v1 = BF(__builtin_amdgcn_raw_buffer_load_b128(rs, l16, vo + 1024, SC1));
        f32x16 s = {}; s = MFMA32(k0, qf[qb][0], s); s = MFMA32(k1, qf[qb][1], s);
        float mb = s[0];
#pragma unroll
        for (int e = 1; e < 16; ++e) mb = fmaxf(mb, s[e]);
        const float mn = fmaxf(m, halfmax(mb)); const float al = __builtin_amdgcn_exp2f(m - mn); m = mn;
#pragma unroll
        for (int e = 0; e < 16; ++e) { s[e] = __builtin_amdgcn_exp2f(s[e] - mn); oo[e] *= al; }
        const u32x4 w0 = {PK(s, 0), PK(s, 2), PK(s, 4), PK(s, 6)}, w1 = {PK(s, 8), PK(s, 10), PK(s, 12), PK(s, 14)};
        float rsum = 0.f;
#pragma unroll
        for (int e = 0; e < 4; ++e) rsum += __uint_as_float(w0[e] << 16) + __uint_as_float(w0[e] & 0xffff0000u) + __uint_as_float(w1[e] << 16) + __uint_as_float(w1[e] & 0xffff0000u);
        { auto rr = __builtin_amdgcn_permlane32_swap(__float_as_uint(rsum), __float_as_uint(rsum), false, false); rsum = __uint_as_float(rr[0]) + __uint_as_float(rr[1]); }
        l = l * al + rsum;
        oo = MFMA32(v0, BF(w0), oo); oo = MFMA32(v1, BF(w1), oo);
      }
      o[qb] = oo;
    }
    const float sc = __uint_as_float(gqu[qb]) / l;
    const unsigned oo2 = WS_A + (unsigned)((r * 8 + wid * 2 + qb) * KS_A + hd * 2) * 1024;
    st16(rs, l16, oo2, pack8(o[qb], 0, sc)); st16(rs, l16, oo2 + 1024, pack8(o[qb], 8, sc));
  }
  WAIT_VM(0);
  if (lds_arrive(ldsb + SYNC + 8, lane) == 3 && lane == 0) flag_set(ws, WS_FO + (unsigned)(r * 32 + hd) * 8, epoch);
  STAMP_AT(3); STAMP_END();
  asm volatile("s_barrier" ::: "memory");
}

DI u64 path_gemm(LAS char* lds, const char* x, const char* wqkv, const char* wo, const char* wg, const char* wu, const char* wd, const char* gatew, char* ws, int w, int j, int r, int tm, int tn, int wid, int lane) {
  using namespace lm;
  f32x4 acc1[4][10];
  const u64 epoch = p1_loop<1>(acc1, lds, x, wqkv, wo, wg, wu, wd, gatew, ws, w, j, r, wid, lane);
  const __amdgpu_buffer_rsrc_t rs = __builtin_amdgcn_make_buffer_rsrc(ws, 0, WS_END, 0x00020000);
  const unsigned ldsb = (unsigned)(uintptr_t)lds, l16 = lane * 16;
  const int kg = wid - 4;
  const int gtok = r * 256 + j * 8 + wid;
  const u32x4* gxp = (const u32x4*)(x + (size_t)gtok * 2048) + lane * 2; const u32x4* ggp = (const u32x4*)gatew + lane * 2;
  const u32x4 ga0 = gxp[0], ga1 = gxp[1], gb0 = ggp[0], gb1 = ggp[1];
  SBAR();
#pragma unroll
  for (int tb = 0; tb < 2; ++tb)
#pragma unroll
    for (int bb = 0; bb < 5; ++bb)
      st16(rs, l16, WS_A + (unsigned)((r * 8 + kg * 2 + tb) * KS_A + KS_O + j * 7 + 2 + bb) * 1024,
           swiglu_frag(acc1[2 * tb][2 * bb], acc1[2 * tb][2 * bb + 1], acc1[2 * tb + 1][2 * bb], acc1[2 * tb + 1][2 * bb + 1]));
  gate_store(ga0, ga1, gb0, gb1, ws, gtok);
  WAIT_VM(0);
  if (lds_arrive(ldsb + SYNC + 4, lane) == 7 && lane == 0) flag_set(ws, WS_FP1 + (unsigned)(r * 32 + j) * 8, epoch);
  STAMP_AT(0);
  flags_wait((gu64*)(ws + WS_FP1) + (tm >> 1) * 32 + (lane & 31), epoch);
  STAMP_AT(1);
  constexpr int D = GW_D, NH_ST = KS_H / 4, NST = KS_A / 4;
  const u64 g2 = __hip_atomic_load((gu64*)(ws + WS_G + (tm * 128 + lane * 2) * 4), RLX_AGENT);
  const unsigned ab = WS_A + (unsigned)(tm * 4) * KS_A * 1024;
  unsigned woff[2];
#pragma unroll
  for (int e = 0; e < 2; ++e) { const int row = e * 8 + (lane >> 3); woff[e] = (unsigned)(row * 2048 + tn * 128 + (((lane & 7) ^ (((row >> 1) & 1) << 2)) << 4)); }
  const unsigned ring = GW + kg * D * 2048;
  bf16x8 a[D][4];
  auto loadw = [&](unsigned dst, int s) {
    const char* wsrc = (s < NH_ST ? wd + (size_t)(4 * s + kg) * 16 * 2048 : wo + (size_t)(4 * (s - NH_ST) + kg) * 16 * 2048);
    glds16(wsrc, woff[0], ldsb + dst); glds16(wsrc, woff[1], ldsb + dst + 1024); };
  auto loada = [&](int slot, int s) {
    const unsigned ks = (unsigned)(s < NH_ST ? KS_O + 4 * s + kg : 4 * (s - NH_ST) + kg);
#pragma unroll
    for (int u = 0; u < 4; ++u) a[slot][u] = BF(__builtin_amdgcn_raw_buffer_load_b128(rs, l16, ab + (u * KS_A + ks) * 1024, SC1)); };
  auto load = [&](int slot, int s) { loadw(ring + slot * 2048, s); loada(slot, s); };
#pragma unroll
  for (int s = 0; s < D; ++s) load(s, s);
  LAS float* gl = (LAS float*)(lds + GL + kg * 512);
  *(LAS f32x2*)(gl + lane * 2) = (f32x2){1.f - __uint_as_float((unsigned)g2), 1.f - __uint_as_float((unsigned)(g2 >> 32))};
  f32x16 acc[4][2];
#pragma unroll
  for (int u = 0; u < 4; ++u)
#pragma unroll
    for (int b = 0; b < 2; ++b)
#pragma unroll
      for (int e = 0; e < 16; ++e) acc[u][b][e] = 0.f;
  const int hi = lane >> 5, q4 = (lane & 15) >> 2, p4 = lane & 3, h16 = (lane >> 4) & 1;
  const unsigned wsw = (unsigned)(((q4 >> 1) & 1) << 6);
  const unsigned wah = (unsigned)((8 * hi + q4) * 128 + 32 * h16 + 8 * p4), wao = (unsigned)((4 * hi + q4) * 128 + 32 * h16 + 8 * p4);
  auto mma = [&](int d, unsigned wsrc, bool oo) {
    asm volatile("" : "+v"(a[d][0]), "+v"(a[d][1]), "+v"(a[d][2]), "+v"(a[d][3]) :: "memory");
    bf16x8 wf[2];
#pragma unroll
    for (int b = 0; b < 2; ++b) { const unsigned p = (((oo ? wao : wah) + b * 64) ^ wsw) + wsrc; wf[b] = tr_frag(lds + p, lds + p + (oo ? 8 : 4) * 128); }
#pragma unroll
    for (int u = 0; u < 4; ++u)
#pragma unroll
      for (int b = 0; b < 2; ++b) acc[u][b] = MFMA32(wf[b], a[d][u], acc[u][b]);
    asm volatile("s_waitcnt lgkmcnt(0)" ::: "memory");
  };
  const int l31 = lane & 31;
#pragma unroll 1
  for (int so = 0; so < 50; so += D) {
#pragma unroll
    for (int d = 0; d < D; ++d) { mma(d, ring + d * 2048, false); SBAR(); load(d, so + d + D); SBAR(); }
  }
  mma(0, ring, false); SBAR(); load(0, 55); SBAR();
#pragma unroll
  for (int d = 1; d < D; ++d) { mma(d, ring + d * 2048, false); SBAR(); }
  mma(0, ring, false);
#pragma unroll
  for (int u = 0; u < 4; ++u) { const float gt = gl[u * 32 + l31];
#pragma unroll
    for (int b = 0; b < 2; ++b)
#pragma unroll
      for (int e = 0; e < 16; ++e) acc[u][b][e] *= gt;
    SBAR(); }
  STAMP_AT(2);
  {
    unsigned v, sv, n;
    asm volatile("s_mov_b32 %2, 0\n1:\n\tds_read_b32 %0, %3\n\ts_waitcnt lgkmcnt(0)\n\tv_readfirstlane_b32 %1, %0\n\ts_add_u32 %2, %2, 1\n\ts_cmp_ge_u32 %1, 4\n\ts_cbranch_scc1 2f\n\ts_sleep 2\n\t"
                 "s_cmp_lt_u32 %2, 0x100000\n\ts_cbranch_scc1 1b\n2:" : "=&v"(v), "=&s"(sv), "=&s"(n) : "v"(ldsb + SYNC + 8) : "memory", "scc");
  }
  auto wpos = [&](int s) { return s < 68 ? (unsigned)(kg * 24576 + (s - 56) * 2048) : ring + (unsigned)(s - 68) * 2048; };
#pragma unroll
  for (int s = 56; s < NST; ++s) loadw(wpos(s), s);
  flags_wait((gu64*)(ws + WS_FO) + (tm >> 1) * 32 + (lane & 31), epoch);
  STAMP_AT(3);
#pragma unroll
  for (int s = 56; s < 56 + D; ++s) loada(s % D, s);
#pragma unroll
  for (int s = 56; s < NST; ++s) { const int d = s % D; mma(d, wpos(s), true); SBAR(); if (s + D < NST) loada(d, s + D); SBAR(); }
  STAMP_AT(4); STAMP_END();
  asm volatile("s_barrier" ::: "memory");
  int tz = threadIdx.x; asm volatile("" : "+v"(tz));
  LAS f32x4* red = (LAS f32x4*)lds + (tz & 63);
#pragma unroll
  for (int u = 0; u < 4; ++u)
#pragma unroll
    for (int b = 0; b < 2; ++b)
#pragma unroll
      for (int e = 0; e < 4; ++e) red[((kg * 8 + u * 2 + b) * 4 + e) * 64] = (f32x4){acc[u][b][4 * e], acc[u][b][4 * e + 1], acc[u][b][4 * e + 2], acc[u][b][4 * e + 3]};
  return epoch;
}

__global__ __launch_bounds__(512, 2) void mega(const char* x, const char* wqkv, const char* wo, const char* wg, const char* wu, const char* wd, const char* gatew, char* ws, char* out) {
  extern __shared__ __attribute__((aligned(16))) char smem[];
  LAS char* lds = (LAS char*)smem;
  const int tid = threadIdx.x, lane = tid & 63, wid = __builtin_amdgcn_readfirstlane(tid >> 6);
  const int w = blockIdx.x, xcd = w & 7, i = w >> 3, j = xcd * 4 + (i & 3), r = i >> 2;
  const int tm = (xcd >> 1) * 4 + (i & 3), tn = (xcd & 1) * 8 + (i >> 2);
  if (tid < 4) *(LAS unsigned*)(lds + lm::SYNC + tid * 4) = 0u;
  u64 epoch = 0;
  if (wid < 4) path_attn(lds, x, wqkv, wo, wg, wu, wd, gatew, ws, w, j, r, wid, lane);
  else epoch = path_gemm(lds, x, wqkv, wo, wg, wu, wd, gatew, ws, w, j, r, tm, tn, wid, lane);
  __syncthreads();
  {
    int tz = threadIdx.x; asm volatile("" : "+v"(tz));
    const int ln = tz & 63;
    LAS const f32x4* red = (LAS const f32x4*)lds + ln;
    unsigned pk[4][2];
#pragma unroll
    for (int e = 0; e < 4; ++e) { f32x4 s = red[((0 * 8 + wid) * 4 + e) * 64];
#pragma unroll
      for (int g = 1; g < 4; ++g) s += red[((g * 8 + wid) * 4 + e) * 64];
      pk[e][0] = cvtpk(s[0], s[1]); pk[e][1] = cvtpk(s[2], s[3]); }
    const __amdgpu_buffer_rsrc_t ro = __builtin_amdgcn_make_buffer_rsrc(out, 0, SEQ * DM * 2, 0x00020000);
    const unsigned ob = (unsigned)((tm * 128 + (wid >> 1) * 32 + (ln & 31)) * 2048 + (tn * 64 + (wid & 1) * 32) * 2 + (ln >> 5) * 16);
#pragma unroll
    for (int e = 0; e < 4; e += 2) {
      const auto r0 = __builtin_amdgcn_permlane32_swap(pk[e][0], pk[e + 1][0], false, false), r1 = __builtin_amdgcn_permlane32_swap(pk[e][1], pk[e + 1][1], false, false);
      const u32x4 v = {r0[0], r1[0], r0[1], r1[1]};
      __builtin_amdgcn_raw_buffer_store_b128(v, ro, ob + e * 16, 0, 0);
    }
  }
  if (w == 0 && tid == 256) flag_set(ws, WS_E, epoch);
}

extern "C" void kernel_launch(void* const* d_in, const int* in_sizes, int n_in, void* d_out, int out_size, void* d_ws, size_t ws_size, hipStream_t stream) {
  (void)hipFuncSetAttribute((const void*)mega, hipFuncAttributeMaxDynamicSharedMemorySize, lm::BYTES);
  mega<<<256, 512, lm::BYTES, stream>>>((const char*)d_in[0], (const char*)d_in[1], (const char*)d_in[2], (const char*)d_in[4], (const char*)d_in[5], (const char*)d_in[6],
                                        (const char*)d_in[7], (char*)d_ws, (char*)d_out);
}
```

```cpp
#include <hip/hip_runtime.h>
#include <stdint.h>

typedef __bf16 bf16x8 __attribute__((ext_vector_type(8)));
typedef __bf16 bf16x2 __attribute__((ext_vector_type(2)));
typedef short s16x4 __attribute__((ext_vector_type(4)));
typedef float f32x16 __attribute__((ext_vector_type(16)));
typedef float f32x4 __attribute__((ext_vector_type(4)));
typedef float f32x2 __attribute__((ext_vector_type(2)));
typedef unsigned u32x4 __attribute__((ext_vector_type(4)));
typedef unsigned u32x2 __attribute__((ext_vector_type(2)));
typedef unsigned long long u64;
#define LAS __attribute__((address_space(3)))
#define GAS __attribute__((address_space(1)))
#define DI __device__ __forceinline__
#define SBAR() __builtin_amdgcn_sched_barrier(0)
#define PIN(x) asm volatile("" : "+v"(x))
#define MFMA32(a, b, c) __builtin_amdgcn_mfma_f32_32x32x16_bf16((a), (b), (c), 0, 0, 0)
#define MFMA16(a, b, c) __builtin_amdgcn_mfma_f32_16x16x32_bf16((a), (b), (c), 0, 0, 0)
#define RLX_AGENT __ATOMIC_RELAXED, __HIP_MEMORY_SCOPE_AGENT
#define SC1 16
#define OUT_AUX 0

constexpr int SEQ = 2048, DM = 1024, FF = 3584, NH = 32;
constexpr int NT32 = SEQ / 32;
constexpr int KS_O = DM / 16;
constexpr int KS_H = FF / 16;
constexpr int KS_A = KS_O + KS_H;
constexpr unsigned WS_E = 0;
constexpr unsigned WS_FKV = 256;
constexpr unsigned WS_FP1 = WS_FKV + 32 * 32 * 8;
constexpr unsigned WS_FO = WS_FP1 + 256 * 8;
constexpr unsigned WS_G = 16384;
constexpr unsigned WS_K = 32768;
constexpr unsigned WS_V = WS_K + NH * NT32 * 2048;
constexpr unsigned WS_A = WS_V + NH * NT32 * 2048;
constexpr unsigned WS_END = WS_A + NT32 * KS_A * 1024;
static_assert(WS_FO + 256 * 8 <= WS_G, "flags");

DI unsigned cvtpk(float lo, float hi) { f32x2 v = {lo, hi}; bf16x2 b = __builtin_convertvector(v, bf16x2); return __builtin_bit_cast(unsigned, b); }
DI u32x4 pack8(const f32x16& a, int o, float s) {
  u32x4 w; w[0] = cvtpk(a[o + 0] * s, a[o + 1] * s); w[1] = cvtpk(a[o + 2] * s, a[o + 3] * s); w[2] = cvtpk(a[o + 4] * s, a[o + 5] * s); w[3] = cvtpk(a[o + 6] * s, a[o + 7] * s); return w; }
DI bf16x8 tr_frag(LAS const char* p0, LAS const char* p1) {
  s16x4 lo = __builtin_amdgcn_ds_read_tr16_b64_v4i16((LAS s16x4*)p0);
  s16x4 hi = __builtin_amdgcn_ds_read_tr16_b64_v4i16((LAS s16x4*)p1);
  return __builtin_bit_cast(bf16x8, __builtin_shufflevector(lo, hi, 0, 1, 2, 3, 4, 5, 6, 7)); }
DI void glds16(const char* sbase, unsigned voff, unsigned ldst) {
  asm volatile("s_mov_b32 m0, %0\n\ts_nop 0\n\tglobal_load_lds_dwordx4 %1, %2" ::"s"(ldst), "v"(voff), "s"(sbase) : "memory"); }
DI void glds16_sc1(const char* sbase, unsigned voff, unsigned ldst) {
  asm volatile("s_mov_b32 m0, %0\n\ts_nop 0\n\tglobal_load_lds_dwordx4 %1, %2 sc1" ::"s"(ldst), "v"(voff), "s"(sbase) : "memory"); }
DI void glds16c(int on, const char* sbase, unsigned voff, unsigned ldst) {
  asm volatile("s_cmp_lt_i32 %3, 1\n\ts_cbranch_scc1 1f\n\ts_mov_b32 m0, %0\n\ts_nop 0\n\tglobal_load_lds_dwordx4 %1, %2\n1:" ::"s"(ldst), "v"(voff), "s"(sbase), "s"(on) : "memory", "scc"); }
DI void l2_prefetch(const char* sbase, unsigned voff, unsigned ldst) {
  asm volatile("s_mov_b32 m0, %0\n\ts_nop 0\n\tglobal_load_lds_dword %1, %2 sc1" ::"s"(ldst), "v"(voff), "s"(sbase) : "memory"); }
#define STAMP_DECL2(kid)
#define STAMP_NEXT()
#define STAMP_DECL(kid)
#define STAMP_AT(i)
#define STAMP_END()
#define WAIT_VM(n) asm volatile("s_waitcnt vmcnt(" #n ")" ::: "memory")
#define WAITVM_BAR(n) asm volatile("s_waitcnt vmcnt(" #n ") lgkmcnt(0)\n\ts_barrier" ::: "memory")

typedef GAS u64 gu64;
DI void flag_set(char* ws, unsigned off, u64 epoch) { __hip_atomic_store((gu64*)(ws + off), epoch, RLX_AGENT); }
DI void flag_poll(u64 v, const gu64* p, u64 epoch) {
  unsigned n;
  asm volatile("s_mov_b32 %1, 0\n1:\n\tv_cmp_ne_u64 vcc, %2, %0\n\ts_cbranch_vccz 2f\n\ts_add_u32 %1, %1, 1\n\ts_cmp_gt_u32 %1, 0xffff\n\ts_cbranch_scc1 2f\n\ts_sleep 1\n\t"
               "global_load_dwordx2 %0, %3, off sc1\n\ts_waitcnt vmcnt(0)\n\ts_branch 1b\n2:"
               : "+v"(v), "=&s"(n) : "s"(epoch), "v"(p) : "vcc", "scc", "memory");
}
DI void flags_wait(const gu64* p, u64 epoch) { flag_poll(__hip_atomic_load(p, RLX_AGENT), p, epoch); }
DI void st16(__amdgpu_buffer_rsrc_t rs, unsigned voff, unsigned soff, u32x4 v) { __builtin_amdgcn_raw_buffer_store_b128(v, rs, voff + soff, 0, SC1); }

namespace lm {
constexpr int P1_X = 0, P1_QA = 32768, P1_QB = 38912, P1_WG = 45056, P1_WU = 59392, P1_STAGE = 73728, P1_PF = 2 * P1_STAGE;
constexpr int AT_STAGE = 32768, GW = 3 * AT_STAGE, GW_D = 5, GL = GW + 4 * GW_D * 2048;
constexpr int SYNC = 163328, BYTES = 163840;
}

constexpr float QSCALE = 0.17677669529663687f * 1.4426950408889634f;
template <int WC> DI u64 p1_loop(f32x4 (&acc)[4][10], LAS char* lds, const char* x, const char* wqkv, const char* wo, const char* wg, const char* wu, const char* wd, const char* gatew, char* ws, int w, int j, int r, int wid, int lane) {
  using namespace lm;
  STAMP_DECL(0); STAMP_AT(0);
  const int wr = wid & 3, g4 = lane >> 4, c16 = lane & 15;
  const unsigned ldsb = (unsigned)(uintptr_t)lds;
  const char* xsrc = x + (size_t)(r * 256 + wid * 32) * 2048;
  unsigned xoff[2];
#pragma unroll
  for (int e = 0; e < 2; ++e) xoff[e] = (unsigned)((lane >> 3) * 2048 + (((lane & 7) ^ ((4 * e + (lane >> 4)) & 7)) << 4));
  const char* wsrc[5]; unsigned woff[5], wdst[5], wstep[5];
#pragma unroll
  for (int m = 0; m < 5; ++m) {
    const int p = wid + 8 * m; const bool isq = p < 12, isa = p < 6, isg = p < 26;
    const int pp = isq ? (isa ? p : p - 6) : (isg ? p - 12 : p - 26);
    wsrc[m] = isq ? wqkv : (isg ? wg : wu);
    wdst[m] = (unsigned)__builtin_amdgcn_readfirstlane((isq ? (isa ? P1_QA : P1_QB) : (isg ? P1_WG : P1_WU)) + pp * 1024);
    wstep[m] = isq ? 64 * 6144 : 64 * 7168;
    const int cc = 64 * pp + lane, rq = cc / 6, pq = cc % 6, rg = cc / 14, pg = cc % 14;
    const int colq = isa ? (pq < 4 ? pq * 8 : 1024 + (pq - 4) * 8) : (pq < 2 ? 1024 + (2 + pq) * 8 : 2048 + (pq - 2) * 8);
    woff[m] = isq ? (unsigned)((rq ^ ((rq >> 1) & 4)) * 6144 + (colq + j * 32) * 2) : (unsigned)((rg ^ ((rg >> 1) & 4)) * 7168 + (j * 112 + pg * 8) * 2);
  }
#define GLC glds16c
#define P1_DMA_X(on, q, t, st) GLC(on, xsrc + (size_t)(t) * 128 + (q) * 8 * 2048, xoff[(q) & 1], ldsb + (st) + P1_X + (wid * 4 + (q)) * 1024)
#define P1_DMA_W(on, m, t, st) GLC(on, wsrc[m] + (size_t)(t) * wstep[m], woff[m], ldsb + (st) + wdst[m])
  STAMP_AT(6);
#pragma unroll
  for (int q = 0; q < 4; ++q) P1_DMA_X(1, q, 0, 0);
#pragma unroll
  for (int m = 0; m < 5; ++m) P1_DMA_W(1, m, 0, 0);
  STAMP_AT(7);
  asm volatile("s_barrier" ::: "memory");
  STAMP_AT(8);
#pragma unroll
  for (int q = 0; q < 4; ++q) P1_DMA_X(1, q, 1, P1_STAGE);
#pragma unroll
  for (int m = 0; m < 5; ++m) P1_DMA_W(1, m, 1, P1_STAGE);
  unsigned pfoff, pfstep;
  {
    const int jj = j & 3, xg = j >> 2, row = (r * 4 + jj) * 2 + (wid & 1), l6 = lane % 6, l7 = (lane & 31) % 7;
    const unsigned ox = (unsigned)((r * 256 + jj * 64 + wid * 16 + (lane & 15)) * 2048);
    const unsigned oq = (unsigned)(row * 6144 + (l6 >> 1) * 2048 + xg * 256 + (l6 & 1) * 128), og = (unsigned)(row * 7168 + xg * 896 + l7 * 128);
    pfoff = wid < 4 ? ox : (wid < 6 ? oq : og); pfstep = wid < 4 ? 128 : (wid < 6 ? 64 * 6144 : 64 * 7168);
  }
  const char* pfl = (wid >= 6 ? (lane < 32 ? wg : wu) : (wid < 4 ? x : wqkv)) + pfoff;
  const bool pfw = wid == 0 && lane >= 16 && lane < 34;
  if (pfw) { const int f = 36 * w + 2 * (lane - 16), ks = f >> 5, fb = f & 31;
    pfl = (ks < KS_O ? wo + (size_t)ks * 16 * 2048 : wd + (size_t)(ks - KS_O) * 16 * 2048) + fb * 64; pfstep = 2048; }
  auto pf = [&](int t, int tw) { asm volatile("s_mov_b32 m0, %0\n\ts_nop 0\n\tglobal_load_lds_dword %1, off sc1" ::"s"(ldsb + P1_PF), "v"(pfl + (size_t)(unsigned)((pfw ? tw : t) * pfstep)) : "memory"); };
  unsigned xa[2];
#pragma unroll
  for (int ks = 0; ks < 2; ++ks) xa[ks] = (unsigned)(P1_X + (wr * 64 + c16) * 128 + (((4 * ks + g4) ^ ((c16 >> 1) & 7)) << 4));
  unsigned qa[2], ga[2];
#pragma unroll
  for (int t = 0; t < 2; ++t) { const int prow = 8 * g4 + 4 * (t ^ (g4 & 1)) + (c16 >> 2);
    qa[t] = (unsigned)(P1_QA + prow * 96 + 8 * (c16 & 3)); ga[t] = (unsigned)(P1_WG + prow * 224 + 8 * (c16 & 3)); }
#pragma unroll
  for (int u = 0; u < 4; ++u)
#pragma unroll
    for (int b = 0; b < 10; ++b) acc[u][b] = (f32x4){0.f, 0.f, 0.f, 0.f};
  bf16x8 xf[2][4]; s16x4 wlo[4], whi[4];
  auto ldx = [&](int u, unsigned st, int ks) { xf[ks][u] = *(LAS const bf16x8*)(lds + st + xa[ks] + u * 2048); };
  auto ldw = [&](int b, unsigned st, int ks, int h) {
    const int slot = (10 * ks + b) & 3;
    unsigned a;
    if (WC == 0 && b < 6) a = qa[h] + ks * 32 * 96 + (b < 3 ? b * 32 : (P1_QB - P1_QA) + (b - 3) * 32);
    else { const int gb = WC == 0 ? (b - 6) >> 1 : 2 + (b >> 1); a = ga[h] + ks * 32 * 224 + gb * 32 + ((b & 1) ? P1_WU - P1_WG : 0); }
    const s16x4 v = __builtin_amdgcn_ds_read_tr16_b64_v4i16((LAS s16x4*)(lds + st + a));
    if (h == 0) wlo[slot] = v; else whi[slot] = v; };
  auto mma1 = [&](int b, int ks, int u) {
    const int slot = (10 * ks + b) & 3;
    const bf16x8 wf = __builtin_bit_cast(bf16x8, __builtin_shufflevector(wlo[slot], whi[slot], 0, 1, 2, 3, 4, 5, 6, 7));
    if (WC == 0 && (b == 4 || b == 5)) acc[u][b] = MFMA16(xf[ks][u], wf, acc[u][b]); else acc[u][b] = MFMA16(wf, xf[ks][u], acc[u][b]); };
#define NOP (void)0
  pf(2, 0); pf(3, 0);
  const u64 e0 = __hip_atomic_load((gu64*)(ws + WS_E), RLX_AGENT);
  l2_prefetch(gatew, (unsigned)(lane & 15) * 128, ldsb + P1_PF);
#define P1W0 "13"
#define P1W1 "5"
  STAMP_AT(9);
  asm volatile("s_waitcnt vmcnt(" P1W0 ")\n\ts_barrier" ::: "memory");
  STAMP_AT(1);
#pragma unroll
  for (int u = 0; u < 4; ++u) ldx(u, 0, 0);
#pragma unroll
  for (int b = 0; b < 3; ++b) { ldw(b, 0, 0, 0); ldw(b, 0, 0, 1); }
  unsigned cur = 0;
#pragma unroll 1
  for (int t = 0; t < 16; ++t) {
    const unsigned nxt = P1_STAGE - cur;
    const int m1 = t * (15 - t), m2 = 14 - t;
    const int tp = t + 4 < 16 ? t + 4 : 15;
#define P1_GRP(b, ks, R0, R1, R2, R3) do { mma1(b, ks, 0); SBAR(); R0; SBAR(); mma1(b, ks, 1); SBAR(); R1; SBAR(); mma1(b, ks, 2); SBAR(); R2; SBAR(); mma1(b, ks, 3); SBAR(); R3; SBAR(); } while (0)
#define LDW_AHEAD0(b, h) do { if ((b) + 3 < 10) ldw((b) + 3, cur, 0, h); else ldw((b) + 3 - 10, cur, 1, h); } while (0)
#define P1_G0(b, SIDE) P1_GRP(b, 0, LDW_AHEAD0(b, 0), LDW_AHEAD0(b, 1), if ((b) < 4) ldx(b, cur, 1), SIDE)
    P1_G0(0, P1_DMA_X(m1, 3, t + 1, nxt)); P1_G0(1, P1_DMA_W(m1, 0, t + 1, nxt)); P1_G0(2, P1_DMA_W(m1, 1, t + 1, nxt)); P1_G0(3, P1_DMA_W(m1, 2, t + 1, nxt));
    P1_G0(4, P1_DMA_W(m1, 3, t + 1, nxt)); P1_G0(5, P1_DMA_W(m1, 4, t + 1, nxt)); P1_G0(6, pf(tp, t)); P1_G0(7, NOP); P1_G0(8, NOP); P1_G0(9, NOP);
#define P1_G1(b) P1_GRP(b, 1, ldw((b) + 3, cur, 1, 0), ldw((b) + 3, cur, 1, 1), NOP, NOP)
    P1_G1(0); P1_G1(1); P1_G1(2); P1_G1(3); P1_G1(4); P1_G1(5); P1_G1(6);
    asm volatile("s_cmp_lg_u32 %0, 0\n\ts_cbranch_scc1 1f\n\ts_waitcnt vmcnt(" P1W1 ") lgkmcnt(0)\n\ts_branch 2f\n1:\n\ts_waitcnt vmcnt(1) lgkmcnt(0)\n2:\n\ts_barrier" ::"s"(t) : "memory", "scc");
    P1_GRP(7, 1, ldx(0, nxt, 0); ldw(0, nxt, 0, 0), ldx(1, nxt, 0); ldw(0, nxt, 0, 1), ldx(2, nxt, 0), ldx(3, nxt, 0); P1_DMA_X(m2, 0, t + 2, cur));
    P1_GRP(8, 1, ldw(1, nxt, 0, 0), ldw(1, nxt, 0, 1), NOP, P1_DMA_X(m2, 1, t + 2, cur));
    P1_GRP(9, 1, ldw(2, nxt, 0, 0), ldw(2, nxt, 0, 1), NOP, P1_DMA_X(m2, 2, t + 2, cur));
    cur = nxt;
  }
  WAITVM_BAR(0);
  STAMP_AT(2);
  return (((u64)(unsigned)__builtin_amdgcn_readfirstlane((unsigned)(e0 >> 32)) << 32) | (unsigned)__builtin_amdgcn_readfirstlane((unsigned)e0)) + 1;
}

DI u32x4 frag16(const f32x4& lo, const f32x4& hi, float s) {
  const auto r0 = __builtin_amdgcn_permlane16_swap(cvtpk(lo[0] * s, lo[1] * s), cvtpk(hi[0] * s, hi[1] * s), false, false);
  const auto r1 = __builtin_amdgcn_permlane16_swap(cvtpk(lo[2] * s, lo[3] * s), cvtpk(hi[2] * s, hi[3] * s), false, false);
  return (u32x4){r0[0], r1[0], r0[1], r1[1]}; }
DI u32x4 swiglu_frag(const f32x4& g0, const f32x4& u0, const f32x4& g1, const f32x4& u1) {
  f32x4 h0, h1;
#pragma unroll
  for (int i = 0; i < 4; ++i) { h0[i] = g0[i] * __builtin_amdgcn_rcpf(1.f + __builtin_amdgcn_exp2f(-1.4426950408889634f * g0[i])) * u0[i];
                                h1[i] = g1[i] * __builtin_amdgcn_rcpf(1.f + __builtin_amdgcn_exp2f(-1.4426950408889634f * g1[i])) * u1[i]; }
  return frag16(h0, h1, 1.f); }
DI void gate_store(const u32x4& a0, const u32x4& a1, const u32x4& b0, const u32x4& b1, char* ws, int tok) {
  float s = 0.f;
#pragma unroll
  for (int e = 0; e < 4; ++e) { const unsigned ua0 = a0[e], ub0 = b0[e], ua1 = a1[e], ub1 = b1[e];
    s = __builtin_amdgcn_fdot2_f32_bf16(__builtin_bit_cast(bf16x2, ua0), __builtin_bit_cast(bf16x2, ub0), s, false);
    s = __builtin_amdgcn_fdot2_f32_bf16(__builtin_bit_cast(bf16x2, ua1), __builtin_bit_cast(bf16x2, ub1), s, false); }
#pragma unroll
  for (int o = 32; o >= 1; o >>= 1) s += __shfl_xor(s, o);
  const float g = __builtin_amdgcn_rcpf(1.f + __builtin_amdgcn_exp2f(-1.4426950408889634f * s));
  __hip_atomic_store((GAS unsigned*)(ws + WS_G + tok * 4), __float_as_uint(g), RLX_AGENT);
}
DI unsigned lds_arrive(unsigned addr, int lane) {
  unsigned one = lane == 0 ? 1u : 0u, old;
  asm volatile("s_waitcnt lgkmcnt(0)\n\tds_add_rtn_u32 %0, %1, %2\n\ts_waitcnt lgkmcnt(0)" : "=&v"(old) : "v"(addr), "v"(one) : "memory");
  return (unsigned)__builtin_amdgcn_readfirstlane(old);
}
DI void lds_meet(unsigned addr, int lane, unsigned target) {
  unsigned one = lane == 0 ? 1u : 0u, v, sv, n;
  asm volatile("s_waitcnt lgkmcnt(0)\n\tds_add_u32 %3, %4\n\ts_mov_b32 %2, 0\n1:\n\tds_read_b32 %0, %3\n\ts_waitcnt lgkmcnt(0)\n\tv_readfirstlane_b32 %1, %0\n\ts_add_u32 %2, %2, 1\n\t"
               "s_cmp_ge_u32 %1, %5\n\ts_cbranch_scc1 2f\n\ts_cmp_lt_u32 %2, 0x100000\n\ts_cbranch_scc1 1b\n2:"
               : "=&v"(v), "=&s"(sv), "=&s"(n) : "v"(addr), "v"(one), "s"(target) : "memory", "scc");
}
DI float halfmax(float m) { auto rr = __builtin_amdgcn_permlane32_swap(__float_as_uint(m), __float_as_uint(m), false, false); return fmaxf(__uint_as_float(rr[0]), __uint_as_float(rr[1])); }
#define BF(x) __builtin_bit_cast(bf16x8, x)

DI void path_attn(LAS char* lds, const char* x, const char* wqkv, const char* wo, const char* wg, const char* wu, const char* wd, const char* gatew, char* ws, int w, int hd, int r, int wid, int lane) {
  using namespace lm;
  constexpr int NTILE = 8, STAGE = AT_STAGE;
  f32x4 acc[4][10];
  const u64 epoch = p1_loop<0>(acc, lds, x, wqkv, wo, wg, wu, wd, gatew, ws, w, hd, r, wid, lane);
  STAMP_DECL2(0);
  const __amdgpu_buffer_rsrc_t rs = __builtin_amdgcn_make_buffer_rsrc(ws, 0, WS_END, 0x00020000);
  const unsigned ldsb = (unsigned)(uintptr_t)lds, l16 = lane * 16;
  const int g4 = lane >> 4, c16 = lane & 15;
  const int gtok = r * 256 + hd * 8 + wid;
  const u32x4* gxp = (const u32x4*)(x + (size_t)gtok * 2048) + lane * 2; const u32x4* ggp = (const u32x4*)gatew + lane * 2;
  const u32x4 ga0 = gxp[0], ga1 = gxp[1], gb0 = ggp[0], gb1 = ggp[1];
  SBAR();
  bf16x8 qf[2][2];
  {
    const unsigned vl16 = (unsigned)(((g4 & 1) * 32 + (g4 >> 1) * 16 + c16) * 16);
#pragma unroll
    for (int tb = 0; tb < 2; ++tb) {
      const int tl = wid * 2 + tb, T = r * 8 + tl;
      const unsigned go = (unsigned)(hd * NT32 + T) * 2048;
      LAS char* kl = lds + tl * 2048;
#pragma unroll
      for (int t = 0; t < 2; ++t) {
        const u32x4 kf = frag16(acc[2 * tb][2 + t], acc[2 * tb + 1][2 + t], 1.f);
        st16(rs, l16, WS_K + go + t * 1024, kf); *(LAS u32x4*)(kl + t * 1024 + l16) = kf;
        const f32x4 &v0 = acc[2 * tb + t][4], &v1 = acc[2 * tb + t][5];
        const auto s0 = __builtin_amdgcn_permlane32_swap(cvtpk(v0[0], v0[1]), cvtpk(v1[0], v1[1]), false, false);
        const auto s1 = __builtin_amdgcn_permlane32_swap(cvtpk(v0[2], v0[3]), cvtpk(v1[2], v1[3]), false, false);
        const u32x4 vf = {s0[0], s1[0], s0[1], s1[1]};
        st16(rs, vl16, WS_V + go + t * 1024, vf); *(LAS u32x4*)(kl + 16384 + t * 1024 + vl16) = vf;
        qf[tb][t] = BF(frag16(acc[2 * tb][t], acc[2 * tb + 1][t], QSCALE));
      }
    }
  }
  SBAR();
#pragma unroll
  for (int tb = 0; tb < 2; ++tb)
#pragma unroll
    for (int gb = 0; gb < 2; ++gb)
      st16(rs, l16, WS_A + (unsigned)((r * 8 + wid * 2 + tb) * KS_A + KS_O + hd * 7 + gb) * 1024,
           swiglu_frag(acc[2 * tb][6 + 2 * gb], acc[2 * tb][7 + 2 * gb], acc[2 * tb + 1][6 + 2 * gb], acc[2 * tb + 1][7 + 2 * gb]));
  gate_store(ga0, ga1, gb0, gb1, ws, gtok);
  SBAR();
  STAMP_AT(3);
  const char* kbase = ws + WS_K + (size_t)hd * NT32 * 2048; const char* vbase = ws + WS_V + (size_t)hd * NT32 * 2048;
#define P2_DMA1(e, t, st) glds16_sc1(((e) < 4 ? kbase : vbase) + (size_t)((r + (t)) & 7) * 16384 + wid * 4096 + ((e) & 3) * 1024, l16, ldsb + (st) + ((e) >> 2) * 16384 + wid * 4096 + ((e) & 3) * 1024)
  unsigned meet = 4;
  lds_meet(ldsb + SYNC, lane, meet);
  STAMP_AT(4); STAMP_END();
  STAMP_NEXT(); STAMP_AT(0);
  LAS const char* lp = lds + lane * 16;
  LAS const char* lp2 = lp + 2 * STAGE; asm volatile("" : "+v"(lp2));
#define LPS(st) ((st) >= 2 * STAGE ? lp2 - 2 * STAGE : lp)
#define KFR(st, b, h) (*(LAS const bf16x8*)(LPS(st) + (st) + (b) * 2048 + (h) * 1024))
#define VFR(st, b, h) (*(LAS const bf16x8*)(LPS(st) + (st) + 16384 + (b) * 2048 + (h) * 1024))
  f32x16 negm;
  {
    float m = -INFINITY;
#pragma unroll
    for (int b = 0; b < 4; ++b)
#pragma unroll
      for (int qb = 0; qb < 2; ++qb) {
        f32x16 s = {}; s = MFMA32(KFR(0, b, 0), qf[qb][0], s); s = MFMA32(KFR(0, b, 1), qf[qb][1], s);
#pragma unroll
        for (int e = 0; e < 16; ++e) m = fmaxf(m, s[e]);
      }
    m = halfmax(m);
#pragma unroll
    for (int e = 0; e < 16; ++e) negm[e] = -m;
    PIN(negm);
  }
  bf16x8 ones; { const bool on = ((lane & 1) == ((lane >> 4) & 1)); const __bf16 o = on ? (__bf16)1.0f : (__bf16)0.0f;
#pragma unroll
    for (int e = 0; e < 8; ++e) ones[e] = o; }
  PIN(ones);
  f32x16 o[2] = {}; f32x4 ls[2] = {}; f32x2 lv[2] = {};
  f32x16 sA[2], sB[2]; u32x4 pA[2][2], pB[2][2] = {}; bf16x8 kA[2], kB[2], vA[2], vB[2] = {};
#pragma unroll
  for (int qb = 0; qb < 2; ++qb) { sA[qb] = MFMA32(KFR(0, 0, 0), qf[qb][0], negm); sA[qb] = MFMA32(KFR(0, 0, 1), qf[qb][1], sA[qb]); }
  kB[0] = KFR(0, 1, 0); kB[1] = KFR(0, 1, 1);
  WAIT_VM(5);
  if (lane == 0) flag_set(ws, WS_FKV + (unsigned)((hd * 8 + r) * 4 + wid) * 8, epoch);
  STAMP_AT(1);
  auto start_job = [&](int n) {
    if (n == 0) { WAIT_VM(1);
      if (lds_arrive(ldsb + SYNC + 4, lane) == 7 && lane == 0) flag_set(ws, WS_FP1 + (unsigned)(r * 32 + hd) * 8, epoch); }
    if (n == 1) flags_wait((gu64*)(ws + WS_FKV) + (hd * 8 + ((r + 1 + ((lane >> 2) & 1)) & 7)) * 4 + (lane & 3), epoch);
    if (n >= 1 && n <= 4) {
#pragma unroll
      for (int e = 0; e < 4; ++e) P2_DMA1(((n - 1) & 1) * 4 + e, 1 + ((n - 1) >> 1), (1 + ((n - 1) >> 1)) * STAGE); }
  };
#define EX(S, i) S[i] = __builtin_amdgcn_exp2f(S[i])
#define PK(S, i) cvtpk(S[i], S[i + 1])
#define P2_HALF(q, SX, SY, PX, PY, KY, VY, R0, R1, R2, R3) do { \
    SY[q] = MFMA32(KY[0], qf[q][0], negm); SBAR(); R0; EX(SX[q], 0); EX(SX[q], 1); EX(SX[q], 2); EX(SX[q], 3); PIN(SX[q]); SBAR(); \
    o[q] = MFMA32(VY[0], BF(PY[q][0]), o[q]); SBAR(); R1; EX(SX[q], 4); EX(SX[q], 5); EX(SX[q], 6); PX[q][0][0] = PK(SX[q], 0); PX[q][0][1] = PK(SX[q], 2); PIN(SX[q]); PIN(PX[q][0]); SBAR(); \
    ls[q] = MFMA16(ones, BF(PY[q][0]), ls[q]); SBAR(); EX(SX[q], 7); SBAR(); PX[q][0][2] = PK(SX[q], 4); SBAR(); PX[q][0][3] = PK(SX[q], 6); PIN(PX[q][0]); SBAR(); \
    SY[q] = MFMA32(KY[1], qf[q][1], SY[q]); SBAR(); R2; EX(SX[q], 8); EX(SX[q], 9); EX(SX[q], 10); EX(SX[q], 11); PIN(SX[q]); SBAR(); \
    o[q] = MFMA32(VY[1], BF(PY[q][1]), o[q]); SBAR(); R3; EX(SX[q], 12); EX(SX[q], 13); EX(SX[q], 14); PX[q][1][0] = PK(SX[q], 8); PX[q][1][1] = PK(SX[q], 10); PIN(SX[q]); PIN(PX[q][1]); SBAR(); \
    ls[q] = MFMA16(ones, BF(PY[q][1]), ls[q]); SBAR(); EX(SX[q], 15); SBAR(); PX[q][1][2] = PK(SX[q], 12); SBAR(); PX[q][1][3] = PK(SX[q], 14); PIN(PX[q][1]); SBAR(); } while (0)
#define P2_STEP(SX, SY, PX, PY, KX, KY, VX, VY, kst, kb, vst, vb, DMA0, DMA1) do { \
    P2_HALF(0, SX, SY, PX, PY, KY, VY, VX[0] = VFR(vst, vb, 0), VX[1] = VFR(vst, vb, 1), NOP, DMA0); \
    P2_HALF(1, SX, SY, PX, PY, KY, VY, KX[0] = KFR(kst, kb, 0), KX[1] = KFR(kst, kb, 1), NOP, DMA1); } while (0)
#define STEP_A(kst, kb, vst, vb, DMA0, DMA1) P2_STEP(sA, sB, pA, pB, kA, kB, vA, vB, kst, kb, vst, vb, DMA0, DMA1)
#define STEP_B(kst, kb, vst, vb, DMA0, DMA1) P2_STEP(sB, sA, pB, pA, kB, kA, vB, vA, kst, kb, vst, vb, DMA0, DMA1)
  gu64* const pkv = (gu64*)(ws + WS_FKV) + hd * 32 + (lane & 31);
  gu64* const pp1 = (gu64*)(ws + WS_FP1) + r * 32 + (lane & 31);
  u64 fkv = 0, fp1 = 0; unsigned gqu[2] = {0, 0};
#pragma unroll
  for (int t = 0; t < NTILE; ++t) {
    const unsigned st = (t % 3) * STAGE, sn = ((t + 1) % 3) * STAGE, sp = ((t + 2) % 3) * STAGE;
    const bool d1 = t >= 2 && t + 1 < NTILE, d2 = t >= 1 && t + 2 < NTILE;
    STEP_A(st, 2, st, 0, if (d1) P2_DMA1(2, t + 1, sn), if (d1) P2_DMA1(3, t + 1, sn); if (t == 0) start_job(0));
    STEP_B(st, 3, st, 1, if (d1) P2_DMA1(6, t + 1, sn), if (d1) P2_DMA1(7, t + 1, sn); if (t == 0) start_job(1));
    STEP_A(st, 4, st, 2, NOP, if (t == 0) start_job(2)); STEP_B(st, 5, st, 3, NOP, if (t == 0) start_job(3)); STEP_A(st, 6, st, 4, NOP, if (t == 0) start_job(4)); STEP_B(st, 7, st, 5, NOP, NOP);
    if (t < 7) {
      if (t == 0) WAIT_VM(8);
      else WAIT_VM(0);
      if (t == 1) flag_poll(fkv, pkv, epoch);
      if (t == 6) flag_poll(fp1, pp1, epoch);
      meet += 4; lds_meet(ldsb + SYNC, lane, meet);
      if (t == 0) { fkv = __hip_atomic_load(pkv, RLX_AGENT); SBAR(); }
      if (t == 5) { fp1 = __hip_atomic_load(pp1, RLX_AGENT); SBAR(); }
      if (t == 6) {
#pragma unroll
        for (int qb = 0; qb < 2; ++qb) gqu[qb] = __hip_atomic_load((GAS unsigned*)(ws + WS_G + ((r * 8 + wid * 2 + qb) * 32 + (lane & 31)) * 4), RLX_AGENT);
        SBAR(); }
    }
    STEP_A(sn, 0, st, 6, if (d2) P2_DMA1(0, t + 2, sp), if (d2) P2_DMA1(1, t + 2, sp)); STEP_B(sn, 1, st, 7, if (d2) P2_DMA1(4, t + 2, sp), if (d2) P2_DMA1(5, t + 2, sp));
  }
#pragma unroll
  for (int qb = 0; qb < 2; ++qb) {
    o[qb] = MFMA32(vB[0], BF(pB[qb][0]), o[qb]); o[qb] = MFMA32(vB[1], BF(pB[qb][1]), o[qb]);
    ls[qb] = MFMA16(ones, BF(pB[qb][0]), ls[qb]); ls[qb] = MFMA16(ones, BF(pB[qb][1]), ls[qb]);
  }
  STAMP_AT(2);
#pragma unroll
  for (int qb = 0; qb < 2; ++qb) {
    float l = (lane & 16) ? ls[qb][1] : ls[qb][0];
    float amax = 0.f;
#pragma unroll
    for (int e = 0; e < 16; ++e) amax = fmaxf(amax, fabsf(o[qb][e]));
    const bool bad = !(l < 1.0995116e12f) || !(l > 9.094947e-13f) || !(amax < INFINITY);
    if (__builtin_expect(__any(bad), 0)) {
      float m = -INFINITY; l = 0.f;
      f32x16 oo = {};
      for (int b = 0; b < NT32; ++b) {
        const unsigned ko = WS_K + (unsigned)(hd * NT32 + b) * 2048, vo = WS_V + (unsigned)(hd * NT32 + b) * 2048;
        const bf16x8 k0 = BF(__builtin_amdgcn_raw_buffer_load_b128(rs, l16, ko, SC1)), k1 = BF(__builtin_amdgcn_raw_buffer_load_b128(rs, l16, ko + 1024, SC1));
        const bf16x8 v0 = BF(__builtin_amdgcn_raw_buffer_load_b128(rs, l16, vo, SC1)), v1 = BF(__builtin_amdgcn_raw_buffer_load_b128(rs, l16, vo + 1024, SC1));
        f32x16 s = {}; s = MFMA32(k0, qf[qb][0], s); s = MFMA32(k1, qf[qb][1], s);
        float mb = s[0];
#pragma unroll
        for (int e = 1; e < 16; ++e) mb = fmaxf(mb, s[e]);
        const float mn = fmaxf(m, halfmax(mb)); const float al = __builtin_amdgcn_exp2f(m - mn); m = mn;
#pragma unroll
        for (int e = 0; e < 16; ++e) { s[e] = __builtin_amdgcn_exp2f(s[e] - mn); oo[e] *= al; }
        const u32x4 w0 = {PK(s, 0), PK(s, 2), PK(s, 4), PK(s, 6)}, w1 = {PK(s, 8), PK(s, 10), PK(s, 12), PK(s, 14)};
        float rsum = 0.f;
#pragma unroll
        for (int e = 0; e < 4; ++e) rsum += __uint_as_float(w0[e] << 16) + __uint_as_float(w0[e] & 0xffff0000u) + __uint_as_float(w1[e] << 16) + __uint_as_float(w1[e] & 0xffff0000u);
        { auto rr = __builtin_amdgcn_permlane32_swap(__float_as_uint(rsum), __float_as_uint(rsum), false, false); rsum = __uint_as_float(rr[0]) + __uint_as_float(rr[1]); }
        l = l * al + rsum;
        oo = MFMA32(v0, BF(w0), oo); oo = MFMA32(v1, BF(w1), oo);
      }
      o[qb] = oo;
    }
    const float sc = __uint_as_float(gqu[qb]) / l;
    const unsigned oo2 = WS_A + (unsigned)((r * 8 + wid * 2 + qb) * KS_A + hd * 2) * 1024;
    st16(rs, l16, oo2, pack8(o[qb], 0, sc)); st16(rs, l16, oo2 + 1024, pack8(o[qb], 8, sc));
  }
  WAIT_VM(0);
  if (lds_arrive(ldsb + SYNC + 8, lane) == 3 && lane == 0) flag_set(ws, WS_FO + (unsigned)(r * 32 + hd) * 8, epoch);
  STAMP_AT(3); STAMP_END();
  asm volatile("s_barrier" ::: "memory");
}

DI u64 path_gemm(LAS char* lds, const char* x, const char* wqkv, const char* wo, const char* wg, const char* wu, const char* wd, const char* gatew, char* ws, int w, int j, int r, int tm, int tn, int wid, int lane) {
  using namespace lm;
  f32x4 acc1[4][10];
  const u64 epoch = p1_loop<1>(acc1, lds, x, wqkv, wo, wg, wu, wd, gatew, ws, w, j, r, wid, lane);
  const __amdgpu_buffer_rsrc_t rs = __builtin_amdgcn_make_buffer_rsrc(ws, 0, WS_END, 0x00020000);
  const unsigned ldsb = (unsigned)(uintptr_t)lds, l16 = lane * 16;
  const int kg = wid - 4;
  const int gtok = r * 256 + j * 8 + wid;
  const u32x4* gxp = (const u32x4*)(x + (size_t)gtok * 2048) + lane * 2; const u32x4* ggp = (const u32x4*)gatew + lane * 2;
  const u32x4 ga0 = gxp[0], ga1 = gxp[1], gb0 = ggp[0], gb1 = ggp[1];
  SBAR();
#pragma unroll
  for (int tb = 0; tb < 2; ++tb)
#pragma unroll
    for (int bb = 0; bb < 5; ++bb)
      st16(rs, l16, WS_A + (unsigned)((r * 8 + kg * 2 + tb) * KS_A + KS_O + j * 7 + 2 + bb) * 1024,
           swiglu_frag(acc1[2 * tb][2 * bb], acc1[2 * tb][2 * bb + 1], acc1[2 * tb + 1][2 * bb], acc1[2 * tb + 1][2 * bb + 1]));
  gate_store(ga0, ga1, gb0, gb1, ws, gtok);
  WAIT_VM(0);
  if (lds_arrive(ldsb + SYNC + 4, lane) == 7 && lane == 0) flag_set(ws, WS_FP1 + (unsigned)(r * 32 + j) * 8, epoch);
  STAMP_AT(0);
  flags_wait((gu64*)(ws + WS_FP1) + (tm >> 1) * 32 + (lane & 31), epoch);
  STAMP_AT(1);
  constexpr int D = GW_D, NH_ST = KS_H / 4, NST = KS_A / 4;
  const u64 g2 = __hip_atomic_load((gu64*)(ws + WS_G + (tm * 128 + lane * 2) * 4), RLX_AGENT);
  const unsigned ab = WS_A + (unsigned)(tm * 4) * KS_A * 1024;
  unsigned woff[2];
#pragma unroll
  for (int e = 0; e < 2; ++e) { const int row = e * 8 + (lane >> 3); woff[e] = (unsigned)(row * 2048 + tn * 128 + (((lane & 7) ^ (((row >> 1) & 1) << 2)) << 4)); }
  const unsigned ring = GW + kg * D * 2048;
  bf16x8 a[D][4];
  auto loadw = [&](unsigned dst, int s) {
    const char* wsrc = (s < NH_ST ? wd + (size_t)(4 * s + kg) * 16 * 2048 : wo + (size_t)(4 * (s - NH_ST) + kg) * 16 * 2048);
    glds16(wsrc, woff[0], ldsb + dst); glds16(wsrc, woff[1], ldsb + dst + 1024); };
  auto loada = [&](int slot, int s) {
    const unsigned ks = (unsigned)(s < NH_ST ? KS_O + 4 * s + kg : 4 * (s - NH_ST) + kg);
#pragma unroll
    for (int u = 0; u < 4; ++u) a[slot][u] = BF(__builtin_amdgcn_raw_buffer_load_b128(rs, l16, ab + (u * KS_A + ks) * 1024, SC1)); };
  auto load = [&](int slot, int s) { loadw(ring + slot * 2048, s); loada(slot, s); };
#pragma unroll
  for (int s = 0; s < D; ++s) load(s, s);
  LAS float* gl = (LAS float*)(lds + GL + kg * 512);
  *(LAS f32x2*)(gl + lane * 2) = (f32x2){1.f - __uint_as_float((unsigned)g2), 1.f - __uint_as_float((unsigned)(g2 >> 32))};
  f32x16 acc[4][2];
#pragma unroll
  for (int u = 0; u < 4; ++u)
#pragma unroll
    for (int b = 0; b < 2; ++b)
#pragma unroll
      for (int e = 0; e < 16; ++e) acc[u][b][e] = 0.f;
  const int hi = lane >> 5, q4 = (lane & 15) >> 2, p4 = lane & 3, h16 = (lane >> 4) & 1;
  const unsigned wsw = (unsigned)(((q4 >> 1) & 1) << 6);
  const unsigned wah = (unsigned)((8 * hi + q4) * 128 + 32 * h16 + 8 * p4), wao = (unsigned)((4 * hi + q4) * 128 + 32 * h16 + 8 * p4);
  auto mma = [&](int d, unsigned wsrc, bool oo) {
    asm volatile("" : "+v"(a[d][0]), "+v"(a[d][1]), "+v"(a[d][2]), "+v"(a[d][3]) :: "memory");
    bf16x8 wf[2];
#pragma unroll
    for (int b = 0; b < 2; ++b) { const unsigned p = (((oo ? wao : wah) + b * 64) ^ wsw) + wsrc; wf[b] = tr_frag(lds + p, lds + p + (oo ? 8 : 4) * 128); }
#pragma unroll
    for (int u = 0; u < 4; ++u)
#pragma unroll
      for (int b = 0; b < 2; ++b) acc[u][b] = MFMA32(wf[b], a[d][u], acc[u][b]);
    asm volatile("s_waitcnt lgkmcnt(0)" ::: "memory");
  };
  const int l31 = lane & 31;
#pragma unroll 1
  for (int so = 0; so < 50; so += D) {
#pragma unroll
    for (int d = 0; d < D; ++d) { mma(d, ring + d * 2048, false); SBAR(); load(d, so + d + D); SBAR(); }
  }
  mma(0, ring, false); SBAR(); load(0, 55); SBAR();
#pragma unroll
  for (int d = 1; d < D; ++d) { mma(d, ring + d * 2048, false); SBAR(); }
  mma(0, ring, false);
#pragma unroll
  for (int u = 0; u < 4; ++u) { const float gt = gl[u * 32 + l31];
#pragma unroll
    for (int b = 0; b < 2; ++b)
#pragma unroll
      for (int e = 0; e < 16; ++e) acc[u][b][e] *= gt;
    SBAR(); }
  STAMP_AT(2);
  {
    unsigned v, sv, n;
    asm volatile("s_mov_b32 %2, 0\n1:\n\tds_read_b32 %0, %3\n\ts_waitcnt lgkmcnt(0)\n\tv_readfirstlane_b32 %1, %0\n\ts_add_u32 %2, %2, 1\n\ts_cmp_ge_u32 %1, 4\n\ts_cbranch_scc1 2f\n\ts_sleep 2\n\t"
                 "s_cmp_lt_u32 %2, 0x100000\n\ts_cbranch_scc1 1b\n2:" : "=&v"(v), "=&s"(sv), "=&s"(n) : "v"(ldsb + SYNC + 8) : "memory", "scc");
  }
  auto sof = [&](int n) { return NH_ST + 2 * (n & 7) + (((n >> 3) ^ (kg >> 1)) & 1); };
  auto wpos = [&](int n) { return n < 12 ? (unsigned)(kg * 24576 + n * 2048) : ring + (unsigned)(n - 12) * 2048; };
#pragma unroll
  for (int n = 0; n < 16; ++n) loadw(wpos(n), sof(n));
  {
    const int hq = lane & 31; const bool early = (hq & 3) == 0 || (hq & 3) == 3;
    gu64* pfo = (gu64*)(ws + WS_FO) + (tm >> 1) * 32 + hq; gu64* done = (gu64*)(ws + WS_FP1) + (tm >> 1) * 32 + hq;
    flags_wait(early ? pfo : done, epoch);
    STAMP_AT(3);
#pragma unroll
    for (int n = 0; n < D; ++n) loada(n % D, sof(n));
#pragma unroll
    for (int n = 0; n < 16; ++n) { const int d = n % D; mma(d, wpos(n), true); SBAR();
      if (n + D == 8) flags_wait(pfo, epoch);
      if (n + D < 16) loada(d, sof(n + D));
      SBAR(); }
  }
  STAMP_AT(4); STAMP_END();
  asm volatile("s_barrier" ::: "memory");
  int tz = threadIdx.x; asm volatile("" : "+v"(tz));
  LAS f32x4* red = (LAS f32x4*)lds + (tz & 63);
#pragma unroll
  for (int u = 0; u < 4; ++u)
#pragma unroll
    for (int b = 0; b < 2; ++b)
#pragma unroll
      for (int e = 0; e < 4; ++e) red[((kg * 8 + u * 2 + b) * 4 + e) * 64] = (f32x4){acc[u][b][4 * e], acc[u][b][4 * e + 1], acc[u][b][4 * e + 2], acc[u][b][4 * e + 3]};
  return epoch;
}

__global__ __launch_bounds__(512, 2) void mega(const char* x, const char* wqkv, const char* wo, const char* wg, const char* wu, const char* wd, const char* gatew, char* ws, char* out) {
  extern __shared__ __attribute__((aligned(16))) char smem[];
  LAS char* lds = (LAS char*)smem;
  const int tid = threadIdx.x, lane = tid & 63, wid = __builtin_amdgcn_readfirstlane(tid >> 6);
  const int w = blockIdx.x, xcd = w & 7, i = w >> 3, j = xcd * 4 + (i & 3), r = i >> 2;
  const int tm = (xcd >> 1) * 4 + (i & 3), tn = (xcd & 1) * 8 + (i >> 2);
  if (tid < 4) *(LAS unsigned*)(lds + lm::SYNC + tid * 4) = 0u;
  u64 epoch = 0;
  if (wid < 4) path_attn(lds, x, wqkv, wo, wg, wu, wd, gatew, ws, w, j, r, wid, lane);
  else epoch = path_gemm(lds, x, wqkv, wo, wg, wu, wd, gatew, ws, w, j, r, tm, tn, wid, lane);
  __syncthreads();
  {
    int tz = threadIdx.x; asm volatile("" : "+v"(tz));
    const int ln = tz & 63;
    LAS const f32x4* red = (LAS const f32x4*)lds + ln;
    unsigned pk[4][2];
#pragma unroll
    for (int e = 0; e < 4; ++e) { f32x4 s = red[((0 * 8 + wid) * 4 + e) * 64];
#pragma unroll
      for (int g = 1; g < 4; ++g) s += red[((g * 8 + wid) * 4 + e) * 64];
      pk[e][0] = cvtpk(s[0], s[1]); pk[e][1] = cvtpk(s[2], s[3]); }
    const __amdgpu_buffer_rsrc_t ro = __builtin_amdgcn_make_buffer_rsrc(out, 0, SEQ * DM * 2, 0x00020000);
    const unsigned ob = (unsigned)((tm * 128 + (wid >> 1) * 32 + (ln & 31)) * 2048 + (tn * 64 + (wid & 1) * 32) * 2 + (ln >> 5) * 16);
#pragma unroll
    for (int e = 0; e < 4; e += 2) {
      const auto r0 = __builtin_amdgcn_permlane32_swap(pk[e][0], pk[e + 1][0], false, false), r1 = __builtin_amdgcn_permlane32_swap(pk[e][1], pk[e + 1][1], false, false);
      const u32x4 v = {r0[0], r1[0], r0[1], r1[1]};
      __builtin_amdgcn_raw_buffer_store_b128(v, ro, ob + e * 16, 0, 0);
    }
  }
  if (w == 0 && tid == 256) flag_set(ws, WS_E, epoch);
}

extern "C" void kernel_launch(void* const* d_in, const int* in_sizes, int n_in, void* d_out, int out_size, void* d_ws, size_t ws_size, hipStream_t stream) {
  (void)hipFuncSetAttribute((const void*)mega, hipFuncAttributeMaxDynamicSharedMemorySize, lm::BYTES);
  mega<<<256, 512, lm::BYTES, stream>>>((const char*)d_in[0], (const char*)d_in[1], (const char*)d_in[2], (const char*)d_in[4], (const char*)d_in[5], (const char*)d_in[6],
                                        (const char*)d_in[7], (char*)d_ws, (char*)d_out);
}
```

```cpp
#include <hip/hip_runtime.h>
#include <stdint.h>

typedef __bf16 bf16x8 __attribute__((ext_vector_type(8)));
typedef __bf16 bf16x2 __attribute__((ext_vector_type(2)));
typedef short s16x4 __attribute__((ext_vector_type(4)));
typedef float f32x16 __attribute__((ext_vector_type(16)));
typedef float f32x4 __attribute__((ext_vector_type(4)));
typedef float f32x2 __attribute__((ext_vector_type(2)));
typedef unsigned u32x4 __attribute__((ext_vector_type(4)));
typedef unsigned u32x2 __attribute__((ext_vector_type(2)));
typedef unsigned long long u64;
#define LAS __attribute__((address_space(3)))
#define GAS __attribute__((address_space(1)))
#define DI __device__ __forceinline__
#define SBAR() __builtin_amdgcn_sched_barrier(0)
#define PIN(x) asm volatile("" : "+v"(x))
#define MFMA32(a, b, c) __builtin_amdgcn_mfma_f32_32x32x16_bf16((a), (b), (c), 0, 0, 0)
#define MFMA16(a, b, c) __builtin_amdgcn_mfma_f32_16x16x32_bf16((a), (b), (c), 0, 0, 0)
#define RLX_AGENT __ATOMIC_RELAXED, __HIP_MEMORY_SCOPE_AGENT
#define SC1 16

constexpr int SEQ = 2048, DM = 1024, FF = 3584, NH = 32;
constexpr int NT32 = SEQ / 32;
constexpr int KS_O = DM / 16;
constexpr int KS_H = FF / 16;
constexpr int KS_A = KS_O + KS_H;
constexpr unsigned WS_E = 0;
constexpr unsigned WS_FKV = 256;
constexpr unsigned WS_FP1 = WS_FKV + 32 * 32 * 8;
constexpr unsigned WS_FO = WS_FP1 + 256 * 8;
constexpr unsigned WS_G = 16384;
constexpr unsigned WS_K = 32768;
constexpr unsigned WS_V = WS_K + NH * NT32 * 2048;
constexpr unsigned WS_A = WS_V + NH * NT32 * 2048;
constexpr unsigned WS_END = WS_A + NT32 * KS_A * 1024;

DI unsigned cvtpk(float lo, float hi) { f32x2 v = {lo, hi}; bf16x2 b = __builtin_convertvector(v, bf16x2); return __builtin_bit_cast(unsigned, b); }
DI u32x4 pack8(const f32x16& a, int o, float s) {
  u32x4 w; w[0] = cvtpk(a[o + 0] * s, a[o + 1] * s); w[1] = cvtpk(a[o + 2] * s, a[o + 3] * s); w[2] = cvtpk(a[o + 4] * s, a[o + 5] * s); w[3] = cvtpk(a[o + 6] * s, a[o + 7] * s); return w; }
DI bf16x8 tr_frag(LAS const char* p0, LAS const char* p1) {
  s16x4 lo = __builtin_amdgcn_ds_read_tr16_b64_v4i16((LAS s16x4*)p0);
  s16x4 hi = __builtin_amdgcn_ds_read_tr16_b64_v4i16((LAS s16x4*)p1);
  return __builtin_bit_cast(bf16x8, __builtin_shufflevector(lo, hi, 0, 1, 2, 3, 4, 5, 6, 7)); }
DI void glds16(const char* sbase, unsigned voff, unsigned ldst) {
  asm volatile("s_mov_b32 m0, %0\n\ts_nop 0\n\tglobal_load_lds_dwordx4 %1, %2" ::"s"(ldst), "v"(voff), "s"(sbase) : "memory"); }
DI void glds16_sc1(const char* sbase, unsigned voff, unsigned ldst) {
  asm volatile("s_mov_b32 m0, %0\n\ts_nop 0\n\tglobal_load_lds_dwordx4 %1, %2 sc1" ::"s"(ldst), "v"(voff), "s"(sbase) : "memory"); }
DI void glds16c(int on, const char* sbase, unsigned voff, unsigned ldst) {
  asm volatile("s_cmp_lt_i32 %3, 1\n\ts_cbranch_scc1 1f\n\ts_mov_b32 m0, %0\n\ts_nop 0\n\tglobal_load_lds_dwordx4 %1, %2\n1:" ::"s"(ldst), "v"(voff), "s"(sbase), "s"(on) : "memory", "scc"); }
DI void l2_prefetch(const char* sbase, unsigned voff, unsigned ldst) {
  asm volatile("s_mov_b32 m0, %0\n\ts_nop 0\n\tglobal_load_lds_dword %1, %2 sc1" ::"s"(ldst), "v"(voff), "s"(sbase) : "memory"); }
#define WAIT_VM(n) asm volatile("s_waitcnt vmcnt(" #n ")" ::: "memory")
#define WAITVM_BAR(n) asm volatile("s_waitcnt vmcnt(" #n ") lgkmcnt(0)\n\ts_barrier" ::: "memory")

typedef GAS u64 gu64;
DI void flag_set(char* ws, unsigned off, u64 epoch) { __hip_atomic_store((gu64*)(ws + off), epoch, RLX_AGENT); }
DI void flag_poll(u64 v, const gu64* p, u64 epoch) {
  unsigned n;
  asm volatile("s_mov_b32 %1, 0\n1:\n\tv_cmp_ne_u64 vcc, %2, %0\n\ts_cbranch_vccz 2f\n\ts_add_u32 %1, %1, 1\n\ts_cmp_gt_u32 %1, 0xffff\n\ts_cbranch_scc1 2f\n\ts_sleep 1\n\t"
               "global_load_dwordx2 %0, %3, off sc1\n\ts_waitcnt vmcnt(0)\n\ts_branch 1b\n2:"
               : "+v"(v), "=&s"(n) : "s"(epoch), "v"(p) : "vcc", "scc", "memory");
}
DI void flags_wait(const gu64* p, u64 epoch) { flag_poll(__hip_atomic_load(p, RLX_AGENT), p, epoch); }
DI void st16(__amdgpu_buffer_rsrc_t rs, unsigned voff, unsigned soff, u32x4 v) { __builtin_amdgcn_raw_buffer_store_b128(v, rs, voff + soff, 0, SC1); }


namespace lm {
constexpr int P1_X = 0, P1_QA = 32768, P1_QB = 38912, P1_WG = 45056, P1_WU = 59392, P1_STAGE = 73728, P1_PF = 2 * P1_STAGE;
constexpr int AT_STAGE = 32768, GW = 3 * AT_STAGE, GW_D = 5, GL = GW + 4 * GW_D * 2048;
constexpr int SYNC = 163328, BYTES = 163840;
}

constexpr float QSCALE = 0.17677669529663687f * 1.4426950408889634f;
template <int WC> DI u64 p1_loop(f32x4 (&acc)[4][10], LAS char* lds, const char* x, const char* wqkv, const char* wo, const char* wg, const char* wu, const char* wd, const char* gatew, char* ws, int w, int j, int r, int wid, int lane) {
  using namespace lm;
  const int wr = wid & 3, g4 = lane >> 4, c16 = lane & 15;
  const unsigned ldsb = (unsigned)(uintptr_t)lds;
  const char* xsrc = x + (size_t)(r * 256 + wid * 32) * 2048;
  unsigned xoff[2];
#pragma unroll
  for (int e = 0; e < 2; ++e) xoff[e] = (unsigned)((lane >> 3) * 2048 + (((lane & 7) ^ ((4 * e + (lane >> 4)) & 7)) << 4));
  const char* wsrc[5]; unsigned woff[5], wdst[5], wstep[5];
#pragma unroll
  for (int m = 0; m < 5; ++m) {
    const int p = wid + 8 * m; const bool isq = p < 12, isa = p < 6, isg = p < 26;
    const int pp = isq ? (isa ? p : p - 6) : (isg ? p - 12 : p - 26);
    wsrc[m] = isq ? wqkv : (isg ? wg : wu);
    wdst[m] = (unsigned)__builtin_amdgcn_readfirstlane((isq ? (isa ? P1_QA : P1_QB) : (isg ? P1_WG : P1_WU)) + pp * 1024);
    wstep[m] = isq ? 64 * 6144 : 64 * 7168;
    const int cc = 64 * pp + lane, rq = cc / 6, pq = cc % 6, rg = cc / 14, pg = cc % 14;
    const int colq = isa ? (pq < 4 ? pq * 8 : 1024 + (pq - 4) * 8) : (pq < 2 ? 1024 + (2 + pq) * 8 : 2048 + (pq - 2) * 8);
    woff[m] = isq ? (unsigned)((rq ^ ((rq >> 1) & 4)) * 6144 + (colq + j * 32) * 2) : (unsigned)((rg ^ ((rg >> 1) & 4)) * 7168 + (j * 112 + pg * 8) * 2);
  }
#define P1_DMA_X(on, q, t, st) glds16c(on, xsrc + (size_t)(t) * 128 + (q) * 8 * 2048, xoff[(q) & 1], ldsb + (st) + P1_X + (wid * 4 + (q)) * 1024)
#define P1_DMA_W(on, m, t, st) glds16c(on, wsrc[m] + (size_t)(t) * wstep[m], woff[m], ldsb + (st) + wdst[m])
#pragma unroll
  for (int q = 0; q < 4; ++q) P1_DMA_X(1, q, 0, 0);
#pragma unroll
  for (int m = 0; m < 5; ++m) P1_DMA_W(1, m, 0, 0);
  asm volatile("s_barrier" ::: "memory");
#pragma unroll
  for (int q = 0; q < 4; ++q) P1_DMA_X(1, q, 1, P1_STAGE);
#pragma unroll
  for (int m = 0; m < 5; ++m) P1_DMA_W(1, m, 1, P1_STAGE);
  unsigned pfoff, pfstep;
  {
    const int jj = j & 3, xg = j >> 2, row = (r * 4 + jj) * 2 + (wid & 1), l6 = lane % 6, l7 = (lane & 31) % 7;
    const unsigned ox = (unsigned)((r * 256 + jj * 64 + wid * 16 + (lane & 15)) * 2048);
    const unsigned oq = (unsigned)(row * 6144 + (l6 >> 1) * 2048 + xg * 256 + (l6 & 1) * 128), og = (unsigned)(row * 7168 + xg * 896 + l7 * 128);
    pfoff = wid < 4 ? ox : (wid < 6 ? oq : og); pfstep = wid < 4 ? 128 : (wid < 6 ? 64 * 6144 : 64 * 7168);
  }
  const char* pfl = (wid >= 6 ? (lane < 32 ? wg : wu) : (wid < 4 ? x : wqkv)) + pfoff;
  const bool pfw = wid == 0 && lane >= 16 && lane < 34;
  if (pfw) { const int f = 36 * w + 2 * (lane - 16), ks = f >> 5, fb = f & 31;
    pfl = (ks < KS_O ? wo + (size_t)ks * 16 * 2048 : wd + (size_t)(ks - KS_O) * 16 * 2048) + fb * 64; pfstep = 2048; }
  auto pf = [&](int t, int tw) { asm volatile("s_mov_b32 m0, %0\n\ts_nop 0\n\tglobal_load_lds_dword %1, off sc1" ::"s"(ldsb + P1_PF), "v"(pfl + (size_t)(unsigned)((pfw ? tw : t) * pfstep)) : "memory"); };
  unsigned xa[2];
#pragma unroll
  for (int ks = 0; ks < 2; ++ks) xa[ks] = (unsigned)(P1_X + (wr * 64 + c16) * 128 + (((4 * ks + g4) ^ ((c16 >> 1) & 7)) << 4));
  unsigned qa[2], ga[2];
#pragma unroll
  for (int t = 0; t < 2; ++t) { const int prow = 8 * g4 + 4 * (t ^ (g4 & 1)) + (c16 >> 2);
    qa[t] = (unsigned)(P1_QA + prow * 96 + 8 * (c16 & 3)); ga[t] = (unsigned)(P1_WG + prow * 224 + 8 * (c16 & 3)); }
#pragma unroll
  for (int u = 0; u < 4; ++u)
#pragma unroll
    for (int b = 0; b < 10; ++b) acc[u][b] = (f32x4){0.f, 0.f, 0.f, 0.f};
  bf16x8 xf[2][4]; s16x4 wlo[4], whi[4];
  auto ldx = [&](int u, unsigned st, int ks) { xf[ks][u] = *(LAS const bf16x8*)(lds + st + xa[ks] + u * 2048); };
  auto ldw = [&](int b, unsigned st, int ks, int h) {
    const int slot = (10 * ks + b) & 3;
    unsigned a;
    if (WC == 0 && b < 6) a = qa[h] + ks * 32 * 96 + (b < 3 ? b * 32 : (P1_QB - P1_QA) + (b - 3) * 32);
    else { const int gb = WC == 0 ? (b - 6) >> 1 : 2 + (b >> 1); a = ga[h] + ks * 32 * 224 + gb * 32 + ((b & 1) ? P1_WU - P1_WG : 0); }
    const s16x4 v = __builtin_amdgcn_ds_read_tr16_b64_v4i16((LAS s16x4*)(lds + st + a));
    if (h == 0) wlo[slot] = v; else whi[slot] = v; };
  auto mma1 = [&](int b, int ks, int u) {
    const int slot = (10 * ks + b) & 3;
    const bf16x8 wf = __builtin_bit_cast(bf16x8, __builtin_shufflevector(wlo[slot], whi[slot], 0, 1, 2, 3, 4, 5, 6, 7));
    if (WC == 0 && (b == 4 || b == 5)) acc[u][b] = MFMA16(xf[ks][u], wf, acc[u][b]); else acc[u][b] = MFMA16(wf, xf[ks][u], acc[u][b]); };
#define NOP (void)0
  pf(2, 0); pf(3, 0);
  const u64 e0 = __hip_atomic_load((gu64*)(ws + WS_E), RLX_AGENT);
  l2_prefetch(gatew, (unsigned)(lane & 15) * 128, ldsb + P1_PF);
  asm volatile("s_waitcnt vmcnt(13)\n\ts_barrier" ::: "memory");
#pragma unroll
  for (int u = 0; u < 4; ++u) ldx(u, 0, 0);
#pragma unroll
  for (int b = 0; b < 3; ++b) { ldw(b, 0, 0, 0); ldw(b, 0, 0, 1); }
  unsigned cur = 0;
#pragma unroll 1
  for (int t = 0; t < 16; ++t) {
    const unsigned nxt = P1_STAGE - cur;
    const int m1 = t * (15 - t), m2 = 14 - t;
    const int tp = t + 4 < 16 ? t + 4 : 15;
#define P1_GRP(b, ks, R0, R1, R2, R3) do { mma1(b, ks, 0); SBAR(); R0; SBAR(); mma1(b, ks, 1); SBAR(); R1; SBAR(); mma1(b, ks, 2); SBAR(); R2; SBAR(); mma1(b, ks, 3); SBAR(); R3; SBAR(); } while (0)
#define LDW_AHEAD0(b, h) do { if ((b) + 3 < 10) ldw((b) + 3, cur, 0, h); else ldw((b) + 3 - 10, cur, 1, h); } while (0)
#define P1_G0(b, SIDE) P1_GRP(b, 0, LDW_AHEAD0(b, 0), LDW_AHEAD0(b, 1), if ((b) < 4) ldx(b, cur, 1), SIDE)
    P1_G0(0, P1_DMA_X(m1, 3, t + 1, nxt)); P1_G0(1, P1_DMA_W(m1, 0, t + 1, nxt)); P1_G0(2, P1_DMA_W(m1, 1, t + 1, nxt)); P1_G0(3, P1_DMA_W(m1, 2, t + 1, nxt));
    P1_G0(4, P1_DMA_W(m1, 3, t + 1, nxt)); P1_G0(5, P1_DMA_W(m1, 4, t + 1, nxt)); P1_G0(6, pf(tp, t)); P1_G0(7, NOP); P1_G0(8, NOP); P1_G0(9, NOP);
#define P1_G1(b) P1_GRP(b, 1, ldw((b) + 3, cur, 1, 0), ldw((b) + 3, cur, 1, 1), NOP, NOP)
    P1_G1(0); P1_G1(1); P1_G1(2); P1_G1(3); P1_G1(4); P1_G1(5); P1_G1(6);
    asm volatile("s_cmp_lg_u32 %0, 0\n\ts_cbranch_scc1 1f\n\ts_waitcnt vmcnt(5) lgkmcnt(0)\n\ts_branch 2f\n1:\n\ts_waitcnt vmcnt(1) lgkmcnt(0)\n2:\n\ts_barrier" ::"s"(t) : "memory", "scc");
    P1_GRP(7, 1, ldx(0, nxt, 0); ldw(0, nxt, 0, 0), ldx(1, nxt, 0); ldw(0, nxt, 0, 1), ldx(2, nxt, 0), ldx(3, nxt, 0); P1_DMA_X(m2, 0, t + 2, cur));
    P1_GRP(8, 1, ldw(1, nxt, 0, 0), ldw(1, nxt, 0, 1), NOP, P1_DMA_X(m2, 1, t + 2, cur));
    P1_GRP(9, 1, ldw(2, nxt, 0, 0), ldw(2, nxt, 0, 1), NOP, P1_DMA_X(m2, 2, t + 2, cur));
    cur = nxt;
  }
  WAITVM_BAR(0);
  return (((u64)(unsigned)__builtin_amdgcn_readfirstlane((unsigned)(e0 >> 32)) << 32) | (unsigned)__builtin_amdgcn_readfirstlane((unsigned)e0)) + 1;
}

DI u32x4 frag16(const f32x4& lo, const f32x4& hi, float s) {
  const auto r0 = __builtin_amdgcn_permlane16_swap(cvtpk(lo[0] * s, lo[1] * s), cvtpk(hi[0] * s, hi[1] * s), false, false);
  const auto r1 = __builtin_amdgcn_permlane16_swap(cvtpk(lo[2] * s, lo[3] * s), cvtpk(hi[2] * s, hi[3] * s), false, false);
  return (u32x4){r0[0], r1[0], r0[1], r1[1]}; }
DI u32x4 swiglu_frag(const f32x4& g0, const f32x4& u0, const f32x4& g1, const f32x4& u1) {
  f32x4 h0, h1;
#pragma unroll
  for (int i = 0; i < 4; ++i) { h0[i] = g0[i] * __builtin_amdgcn_rcpf(1.f + __builtin_amdgcn_exp2f(-1.4426950408889634f * g0[i])) * u0[i];
                                h1[i] = g1[i] * __builtin_amdgcn_rcpf(1.f + __builtin_amdgcn_exp2f(-1.4426950408889634f * g1[i])) * u1[i]; }
  return frag16(h0, h1, 1.f); }
DI void gate_store(const u32x4& a0, const u32x4& a1, const u32x4& b0, const u32x4& b1, char* ws, int tok) {
  float s = 0.f;
#pragma unroll
  for (int e = 0; e < 4; ++e) { const unsigned ua0 = a0[e], ub0 = b0[e], ua1 = a1[e], ub1 = b1[e];
    s = __builtin_amdgcn_fdot2_f32_bf16(__builtin_bit_cast(bf16x2, ua0), __builtin_bit_cast(bf16x2, ub0), s, false);
    s = __builtin_amdgcn_fdot2_f32_bf16(__builtin_bit_cast(bf16x2, ua1), __builtin_bit_cast(bf16x2, ub1), s, false); }
#pragma unroll
  for (int o = 32; o >= 1; o >>= 1) s += __shfl_xor(s, o);
  const float g = __builtin_amdgcn_rcpf(1.f + __builtin_amdgcn_exp2f(-1.4426950408889634f * s));
  __hip_atomic_store((GAS unsigned*)(ws + WS_G + tok * 4), __float_as_uint(g), RLX_AGENT);
}
DI unsigned lds_arrive(unsigned addr, int lane) {
  unsigned one = lane == 0 ? 1u : 0u, old;
  asm volatile("s_waitcnt lgkmcnt(0)\n\tds_add_rtn_u32 %0, %1, %2\n\ts_waitcnt lgkmcnt(0)" : "=&v"(old) : "v"(addr), "v"(one) : "memory");
  return (unsigned)__builtin_amdgcn_readfirstlane(old);
}
DI void lds_meet(unsigned addr, int lane, unsigned target) {
  unsigned one = lane == 0 ? 1u : 0u, v, sv, n;
  asm volatile("s_waitcnt lgkmcnt(0)\n\tds_add_u32 %3, %4\n\ts_mov_b32 %2, 0\n1:\n\tds_read_b32 %0, %3\n\ts_waitcnt lgkmcnt(0)\n\tv_readfirstlane_b32 %1, %0\n\ts_add_u32 %2, %2, 1\n\t"
               "s_cmp_ge_u32 %1, %5\n\ts_cbranch_scc1 2f\n\ts_cmp_lt_u32 %2, 0x100000\n\ts_cbranch_scc1 1b\n2:"
               : "=&v"(v), "=&s"(sv), "=&s"(n) : "v"(addr), "v"(one), "s"(target) : "memory", "scc");
}
DI float halfmax(float m) { auto rr = __builtin_amdgcn_permlane32_swap(__float_as_uint(m), __float_as_uint(m), false, false); return fmaxf(__uint_as_float(rr[0]), __uint_as_float(rr[1])); }
#define BF(x) __builtin_bit_cast(bf16x8, x)

DI void path_attn(LAS char* lds, const char* x, const char* wqkv, const char* wo, const char* wg, const char* wu, const char* wd, const char* gatew, char* ws, int w, int hd, int r, int wid, int lane) {
  using namespace lm;
  constexpr int NTILE = 8, STAGE = AT_STAGE;
  f32x4 acc[4][10];
  const u64 epoch = p1_loop<0>(acc, lds, x, wqkv, wo, wg, wu, wd, gatew, ws, w, hd, r, wid, lane);
  const __amdgpu_buffer_rsrc_t rs = __builtin_amdgcn_make_buffer_rsrc(ws, 0, WS_END, 0x00020000);
  const unsigned ldsb = (unsigned)(uintptr_t)lds, l16 = lane * 16;
  const int g4 = lane >> 4, c16 = lane & 15;
  const int gtok = r * 256 + hd * 8 + wid;
  const u32x4* gxp = (const u32x4*)(x + (size_t)gtok * 2048) + lane * 2; const u32x4* ggp = (const u32x4*)gatew + lane * 2;
  const u32x4 ga0 = gxp[0], ga1 = gxp[1], gb0 = ggp[0], gb1 = ggp[1];
  SBAR();
  bf16x8 qf[2][2];
  {
    const unsigned vl16 = (unsigned)(((g4 & 1) * 32 + (g4 >> 1) * 16 + c16) * 16);
#pragma unroll
    for (int tb = 0; tb < 2; ++tb) {
      const int tl = wid * 2 + tb, T = r * 8 + tl;
      const unsigned go = (unsigned)(hd * NT32 + T) * 2048;
      LAS char* kl = lds + tl * 2048;
#pragma unroll
      for (int t = 0; t < 2; ++t) {
        const u32x4 kf = frag16(acc[2 * tb][2 + t], acc[2 * tb + 1][2 + t], 1.f);
        st16(rs, l16, WS_K + go + t * 1024, kf); *(LAS u32x4*)(kl + t * 1024 + l16) = kf;
        const f32x4 &v0 = acc[2 * tb + t][4], &v1 = acc[2 * tb + t][5];
        const auto s0 = __builtin_amdgcn_permlane32_swap(cvtpk(v0[0], v0[1]), cvtpk(v1[0], v1[1]), false, false);
        const auto s1 = __builtin_amdgcn_permlane32_swap(cvtpk(v0[2], v0[3]), cvtpk(v1[2], v1[3]), false, false);
        const u32x4 vf = {s0[0], s1[0], s0[1], s1[1]};
        st16(rs, vl16, WS_V + go + t * 1024, vf); *(LAS u32x4*)(kl + 16384 + t * 1024 + vl16) = vf;
        qf[tb][t] = BF(frag16(acc[2 * tb][t], acc[2 * tb + 1][t], QSCALE));
      }
    }
  }
  SBAR();
#pragma unroll
  for (int tb = 0; tb < 2; ++tb)
#pragma unroll
    for (int gb = 0; gb < 2; ++gb)
      st16(rs, l16, WS_A + (unsigned)((r * 8 + wid * 2 + tb) * KS_A + KS_O + hd * 7 + gb) * 1024,
           swiglu_frag(acc[2 * tb][6 + 2 * gb], acc[2 * tb][7 + 2 * gb], acc[2 * tb + 1][6 + 2 * gb], acc[2 * tb + 1][7 + 2 * gb]));
  gate_store(ga0, ga1, gb0, gb1, ws, gtok);
  SBAR();
  const char* kbase = ws + WS_K + (size_t)hd * NT32 * 2048; const char* vbase = ws + WS_V + (size_t)hd * NT32 * 2048;
#define P2_DMA1(e, t, st) glds16_sc1(((e) < 4 ? kbase : vbase) + (size_t)((r + (t)) & 7) * 16384 + wid * 4096 + ((e) & 3) * 1024, l16, ldsb + (st) + ((e) >> 2) * 16384 + wid * 4096 + ((e) & 3) * 1024)
  unsigned meet = 4;
  lds_meet(ldsb + SYNC, lane, meet);
  LAS const char* lp = lds + lane * 16;
  LAS const char* lp2 = lp + 2 * STAGE; asm volatile("" : "+v"(lp2));
#define LPS(st) ((st) >= 2 * STAGE ? lp2 - 2 * STAGE : lp)
#define KFR(st, b, h) (*(LAS const bf16x8*)(LPS(st) + (st) + (b) * 2048 + (h) * 1024))
#define VFR(st, b, h) (*(LAS const bf16x8*)(LPS(st) + (st) + 16384 + (b) * 2048 + (h) * 1024))
  f32x16 negm;
  {
    float m = -INFINITY;
#pragma unroll
    for (int b = 0; b < 2; ++b)
#pragma unroll
      for (int qb = 0; qb < 2; ++qb) {
        f32x16 s = {}; s = MFMA32(KFR(0, b, 0), qf[qb][0], s); s = MFMA32(KFR(0, b, 1), qf[qb][1], s);
#pragma unroll
        for (int e = 0; e < 16; ++e) m = fmaxf(m, s[e]);
      }
    m = halfmax(m);
#pragma unroll
    for (int e = 0; e < 16; ++e) negm[e] = -m;
    PIN(negm);
  }
  bf16x8 ones; { const bool on = ((lane & 1) == ((lane >> 4) & 1)); const __bf16 o = on ? (__bf16)1.0f : (__bf16)0.0f;
#pragma unroll
    for (int e = 0; e < 8; ++e) ones[e] = o; }
  PIN(ones);
  f32x16 o[2] = {}; f32x4 ls[2] = {};
  f32x16 sA[2], sB[2]; u32x4 pA[2][2], pB[2][2] = {}; bf16x8 kA[2], kB[2], vA[2], vB[2] = {};
#pragma unroll
  for (int qb = 0; qb < 2; ++qb) { sA[qb] = MFMA32(KFR(0, 0, 0), qf[qb][0], negm); sA[qb] = MFMA32(KFR(0, 0, 1), qf[qb][1], sA[qb]); }
  kB[0] = KFR(0, 1, 0); kB[1] = KFR(0, 1, 1);
  PIN(ls[0]); PIN(ls[1]); PIN(pB[0][0]); PIN(pB[0][1]); PIN(pB[1][0]); PIN(pB[1][1]);
  WAIT_VM(5);
  if (lane == 0) flag_set(ws, WS_FKV + (unsigned)((hd * 8 + r) * 4 + wid) * 8, epoch);
  auto start_job = [&](int n) {
    if (n == 0) { WAIT_VM(1);
      if (lds_arrive(ldsb + SYNC + 4, lane) == 7 && lane == 0) flag_set(ws, WS_FP1 + (unsigned)(r * 32 + hd) * 8, epoch); }
    if (n == 1) flags_wait((gu64*)(ws + WS_FKV) + (hd * 8 + ((r + 1 + ((lane >> 2) & 1)) & 7)) * 4 + (lane & 3), epoch);
    if (n >= 1 && n <= 4) {
#pragma unroll
      for (int e = 0; e < 4; ++e) P2_DMA1(((n - 1) & 1) * 4 + e, 1 + ((n - 1) >> 1), (1 + ((n - 1) >> 1)) * STAGE); }
  };
#define EX(S, i) S[i] = __builtin_amdgcn_exp2f(S[i])
#define PK(S, i) cvtpk(S[i], S[i + 1])
#define SUM16(l, p) asm volatile("v_mfma_f32_16x16x32_bf16 %0, %1, %2, %0" : "+v"(l) : "v"(ones), "v"(p))
#define P2_HALF(q, SX, SY, PX, PY, KY, VY, R0, R1, DMA) do { \
    SY[q] = MFMA32(KY[0], qf[q][0], negm); SBAR(); R0; EX(SX[q], 0); EX(SX[q], 1); EX(SX[q], 2); EX(SX[q], 3); PIN(SX[q]); SBAR(); \
    o[q] = MFMA32(VY[0], BF(PY[q][0]), o[q]); SBAR(); R1; EX(SX[q], 4); EX(SX[q], 5); EX(SX[q], 6); PX[q][0][0] = PK(SX[q], 0); PX[q][0][1] = PK(SX[q], 2); PIN(SX[q]); PIN(PX[q][0]); SBAR(); \
    SUM16(ls[q], PY[q][0]); SBAR(); EX(SX[q], 7); SBAR(); PX[q][0][2] = PK(SX[q], 4); SBAR(); PX[q][0][3] = PK(SX[q], 6); PIN(PX[q][0]); SBAR(); \
    SY[q] = MFMA32(KY[1], qf[q][1], SY[q]); SBAR(); EX(SX[q], 8); EX(SX[q], 9); EX(SX[q], 10); EX(SX[q], 11); PIN(SX[q]); SBAR(); \
    o[q] = MFMA32(VY[1], BF(PY[q][1]), o[q]); SBAR(); DMA; EX(SX[q], 12); EX(SX[q], 13); EX(SX[q], 14); PX[q][1][0] = PK(SX[q], 8); PX[q][1][1] = PK(SX[q], 10); PIN(SX[q]); PIN(PX[q][1]); SBAR(); \
    SUM16(ls[q], PY[q][1]); SBAR(); EX(SX[q], 15); SBAR(); PX[q][1][2] = PK(SX[q], 12); SBAR(); PX[q][1][3] = PK(SX[q], 14); PIN(PX[q][1]); SBAR(); } while (0)
#define P2_STEP(SX, SY, PX, PY, KX, KY, VX, VY, kst, kb, vst, vb, DMA0, DMA1) do { \
    P2_HALF(0, SX, SY, PX, PY, KY, VY, VX[0] = VFR(vst, vb, 0), VX[1] = VFR(vst, vb, 1), DMA0); \
    P2_HALF(1, SX, SY, PX, PY, KY, VY, KX[0] = KFR(kst, kb, 0), KX[1] = KFR(kst, kb, 1), DMA1); } while (0)
#define STEP_A(kst, kb, vst, vb, DMA0, DMA1) P2_STEP(sA, sB, pA, pB, kA, kB, vA, vB, kst, kb, vst, vb, DMA0, DMA1)
#define STEP_B(kst, kb, vst, vb, DMA0, DMA1) P2_STEP(sB, sA, pB, pA, kB, kA, vB, vA, kst, kb, vst, vb, DMA0, DMA1)
  gu64* const pkv = (gu64*)(ws + WS_FKV) + hd * 32 + (lane & 31);
  gu64* const pp1 = (gu64*)(ws + WS_FP1) + r * 32 + (lane & 31);
  u64 fkv = 0, fp1 = 0; unsigned gqu[2] = {0, 0};
#pragma unroll
  for (int t = 0; t < NTILE; ++t) {
    const unsigned st = (t % 3) * STAGE, sn = ((t + 1) % 3) * STAGE, sp = ((t + 2) % 3) * STAGE;
    const bool d1 = t >= 2 && t + 1 < NTILE, d2 = t >= 1 && t + 2 < NTILE;
    STEP_A(st, 2, st, 0, if (d1) P2_DMA1(2, t + 1, sn), if (d1) P2_DMA1(3, t + 1, sn); if (t == 0) start_job(0));
    STEP_B(st, 3, st, 1, if (d1) P2_DMA1(6, t + 1, sn), if (d1) P2_DMA1(7, t + 1, sn); if (t == 0) start_job(1));
    STEP_A(st, 4, st, 2, NOP, if (t == 0) start_job(2)); STEP_B(st, 5, st, 3, NOP, if (t == 0) start_job(3)); STEP_A(st, 6, st, 4, NOP, if (t == 0) start_job(4)); STEP_B(st, 7, st, 5, NOP, NOP);
    if (t < 7) {
      if (t == 0) WAIT_VM(8);
      else WAIT_VM(0);
      if (t == 1) flag_poll(fkv, pkv, epoch);
      if (t == 6) flag_poll(fp1, pp1, epoch);
      meet += 4; lds_meet(ldsb + SYNC, lane, meet);
      if (t == 0) { fkv = __hip_atomic_load(pkv, RLX_AGENT); SBAR(); }
      if (t == 5) { fp1 = __hip_atomic_load(pp1, RLX_AGENT); SBAR(); }
      if (t == 6) {
#pragma unroll
        for (int qb = 0; qb < 2; ++qb) gqu[qb] = __hip_atomic_load((GAS unsigned*)(ws + WS_G + ((r * 8 + wid * 2 + qb) * 32 + (lane & 31)) * 4), RLX_AGENT);
        SBAR(); }
    }
    STEP_A(sn, 0, st, 6, if (d2) P2_DMA1(0, t + 2, sp), if (d2) P2_DMA1(1, t + 2, sp)); STEP_B(sn, 1, st, 7, if (d2) P2_DMA1(4, t + 2, sp), if (d2) P2_DMA1(5, t + 2, sp));
  }
#pragma unroll
  for (int qb = 0; qb < 2; ++qb) {
    o[qb] = MFMA32(vB[0], BF(pB[qb][0]), o[qb]); o[qb] = MFMA32(vB[1], BF(pB[qb][1]), o[qb]);
    ls[qb] = MFMA16(ones, BF(pB[qb][0]), ls[qb]); ls[qb] = MFMA16(ones, BF(pB[qb][1]), ls[qb]);
  }
#pragma unroll
  for (int qb = 0; qb < 2; ++qb) {
    float l = (lane & 16) ? ls[qb][1] : ls[qb][0];
    float amax = 0.f;
#pragma unroll
    for (int e = 0; e < 16; ++e) amax = fmaxf(amax, fabsf(o[qb][e]));
    const bool bad = !(l < 1.0995116e12f) || !(l > 9.094947e-13f) || !(amax < INFINITY);
    if (__builtin_expect(__any(bad), 0)) {
      float m = -INFINITY; l = 0.f;
      f32x16 oo = {};
      for (int b = 0; b < NT32; ++b) {
        const unsigned ko = WS_K + (unsigned)(hd * NT32 + b) * 2048, vo = WS_V + (unsigned)(hd * NT32 + b) * 2048;
        const bf16x8 k0 = BF(__builtin_amdgcn_raw_buffer_load_b128(rs, l16, ko, SC1)), k1 = BF(__builtin_amdgcn_raw_buffer_load_b128(rs, l16, ko + 1024, SC1));
        const bf16x8 v0 = BF(__builtin_amdgcn_raw_buffer_load_b128(rs, l16, vo, SC1)), v1 = BF(__builtin_amdgcn_raw_buffer_load_b128(rs, l16, vo + 1024, SC1));
        f32x16 s = {}; s = MFMA32(k0, qf[qb][0], s); s = MFMA32(k1, qf[qb][1], s);
        float mb = s[0];
#pragma unroll
        for (int e = 1; e < 16; ++e) mb = fmaxf(mb, s[e]);
        const float mn = fmaxf(m, halfmax(mb)); const float al = __builtin_amdgcn_exp2f(m - mn); m = mn;
#pragma unroll
        for (int e = 0; e < 16; ++e) { s[e] = __builtin_amdgcn_exp2f(s[e] - mn); oo[e] *= al; }
        const u32x4 w0 = {PK(s, 0), PK(s, 2), PK(s, 4), PK(s, 6)}, w1 = {PK(s, 8), PK(s, 10), PK(s, 12), PK(s, 14)};
        float rsum = 0.f;
#pragma unroll
        for (int e = 0; e < 4; ++e) rsum += __uint_as_float(w0[e] << 16) + __uint_as_float(w0[e] & 0xffff0000u) + __uint_as_float(w1[e] << 16) + __uint_as_float(w1[e] & 0xffff0000u);
        { auto rr = __builtin_amdgcn_permlane32_swap(__float_as_uint(rsum), __float_as_uint(rsum), false, false); rsum = __uint_as_float(rr[0]) + __uint_as_float(rr[1]); }
        l = l * al + rsum;
        oo = MFMA32(v0, BF(w0), oo); oo = MFMA32(v1, BF(w1), oo);
      }
      o[qb] = oo;
    }
    const float sc = __uint_as_float(gqu[qb]) / l;
    const unsigned oo2 = WS_A + (unsigned)((r * 8 + wid * 2 + qb) * KS_A + hd * 2) * 1024;
    st16(rs, l16, oo2, pack8(o[qb], 0, sc)); st16(rs, l16, oo2 + 1024, pack8(o[qb], 8, sc));
  }
  WAIT_VM(0);
  if (lds_arrive(ldsb + SYNC + 8, lane) == 3 && lane == 0) flag_set(ws, WS_FO + (unsigned)(r * 32 + hd) * 8, epoch);
  asm volatile("s_barrier" ::: "memory");
}

DI u64 path_gemm(LAS char* lds, const char* x, const char* wqkv, const char* wo, const char* wg, const char* wu, const char* wd, const char* gatew, char* ws, int w, int j, int r, int tm, int tn, int wid, int lane) {
  using namespace lm;
  f32x4 acc1[4][10];
  const u64 epoch = p1_loop<1>(acc1, lds, x, wqkv, wo, wg, wu, wd, gatew, ws, w, j, r, wid, lane);
  const __amdgpu_buffer_rsrc_t rs = __builtin_amdgcn_make_buffer_rsrc(ws, 0, WS_END, 0x00020000);
  const unsigned ldsb = (unsigned)(uintptr_t)lds, l16 = lane * 16;
  const int kg = wid - 4;
  const int gtok = r * 256 + j * 8 + wid;
  const u32x4* gxp = (const u32x4*)(x + (size_t)gtok * 2048) + lane * 2; const u32x4* ggp = (const u32x4*)gatew + lane * 2;
  const u32x4 ga0 = gxp[0], ga1 = gxp[1], gb0 = ggp[0], gb1 = ggp[1];
  SBAR();
#pragma unroll
  for (int tb = 0; tb < 2; ++tb)
#pragma unroll
    for (int bb = 0; bb < 5; ++bb)
      st16(rs, l16, WS_A + (unsigned)((r * 8 + kg * 2 + tb) * KS_A + KS_O + j * 7 + 2 + bb) * 1024,
           swiglu_frag(acc1[2 * tb][2 * bb], acc1[2 * tb][2 * bb + 1], acc1[2 * tb + 1][2 * bb], acc1[2 * tb + 1][2 * bb + 1]));
  gate_store(ga0, ga1, gb0, gb1, ws, gtok);
  WAIT_VM(0);
  if (lds_arrive(ldsb + SYNC + 4, lane) == 7 && lane == 0) flag_set(ws, WS_FP1 + (unsigned)(r * 32 + j) * 8, epoch);
  flags_wait((gu64*)(ws + WS_FP1) + (tm >> 1) * 32 + (lane & 31), epoch);
  constexpr int D = GW_D, NH_ST = KS_H / 4;
  const u64 g2 = __hip_atomic_load((gu64*)(ws + WS_G + (tm * 128 + lane * 2) * 4), RLX_AGENT);
  const unsigned ab = WS_A + (unsigned)(tm * 4) * KS_A * 1024;
  unsigned woff[2];
#pragma unroll
  for (int e = 0; e < 2; ++e) { const int row = e * 8 + (lane >> 3); woff[e] = (unsigned)(row * 2048 + tn * 128 + (((lane & 7) ^ (((row >> 1) & 1) << 2)) << 4)); }
  const unsigned ring = GW + kg * D * 2048;
  bf16x8 a[D][4];
  auto loadw = [&](unsigned dst, int s) {
    const char* wsrc = (s < NH_ST ? wd + (size_t)(4 * s + kg) * 16 * 2048 : wo + (size_t)(4 * (s - NH_ST) + kg) * 16 * 2048);
    glds16(wsrc, woff[0], ldsb + dst); glds16(wsrc, woff[1], ldsb + dst + 1024); };
  auto loada = [&](int slot, int s) {
    const unsigned ks = (unsigned)(s < NH_ST ? KS_O + 4 * s + kg : 4 * (s - NH_ST) + kg);
#pragma unroll
    for (int u = 0; u < 4; ++u) a[slot][u] = BF(__builtin_amdgcn_raw_buffer_load_b128(rs, l16, ab + (u * KS_A + ks) * 1024, SC1)); };
  auto load = [&](int slot, int s) { loadw(ring + slot * 2048, s); loada(slot, s); };
#pragma unroll
  for (int s = 0; s < D; ++s) load(s, s);
  LAS float* gl = (LAS float*)(lds + GL + kg * 512);
  *(LAS f32x2*)(gl + lane * 2) = (f32x2){1.f - __uint_as_float((unsigned)g2), 1.f - __uint_as_float((unsigned)(g2 >> 32))};
  f32x16 acc[4][2];
#pragma unroll
  for (int u = 0; u < 4; ++u)
#pragma unroll
    for (int b = 0; b < 2; ++b)
#pragma unroll
      for (int e = 0; e < 16; ++e) acc[u][b][e] = 0.f;
  const int hi = lane >> 5, q4 = (lane & 15) >> 2, p4 = lane & 3, h16 = (lane >> 4) & 1;
  const unsigned wsw = (unsigned)(((q4 >> 1) & 1) << 6);
  const unsigned wah = (unsigned)((8 * hi + q4) * 128 + 32 * h16 + 8 * p4), wao = (unsigned)((4 * hi + q4) * 128 + 32 * h16 + 8 * p4);
  auto mma = [&](int d, unsigned wsrc, bool oo) {
    asm volatile("" : "+v"(a[d][0]), "+v"(a[d][1]), "+v"(a[d][2]), "+v"(a[d][3]) :: "memory");
    bf16x8 wf[2];
#pragma unroll
    for (int b = 0; b < 2; ++b) { const unsigned p = (((oo ? wao : wah) + b * 64) ^ wsw) + wsrc; wf[b] = tr_frag(lds + p, lds + p + (oo ? 8 : 4) * 128); }
#pragma unroll
    for (int u = 0; u < 4; ++u)
#pragma unroll
      for (int b = 0; b < 2; ++b) acc[u][b] = MFMA32(wf[b], a[d][u], acc[u][b]);
    asm volatile("s_waitcnt lgkmcnt(0)" ::: "memory");
  };
  const int l31 = lane & 31;
#pragma unroll 1
  for (int so = 0; so < 50; so += D) {
#pragma unroll
    for (int d = 0; d < D; ++d) { mma(d, ring + d * 2048, false); SBAR(); load(d, so + d + D); SBAR(); }
  }
  mma(0, ring, false); SBAR(); load(0, 55); SBAR();
#pragma unroll
  for (int d = 1; d < D; ++d) { mma(d, ring + d * 2048, false); SBAR(); }
  mma(0, ring, false);
#pragma unroll
  for (int u = 0; u < 4; ++u) { const float gt = gl[u * 32 + l31];
#pragma unroll
    for (int b = 0; b < 2; ++b)
#pragma unroll
      for (int e = 0; e < 16; ++e) acc[u][b][e] *= gt;
    SBAR(); }
  {
    unsigned v, sv, n;
    asm volatile("s_mov_b32 %2, 0\n1:\n\tds_read_b32 %0, %3\n\ts_waitcnt lgkmcnt(0)\n\tv_readfirstlane_b32 %1, %0\n\ts_add_u32 %2, %2, 1\n\ts_cmp_ge_u32 %1, 4\n\ts_cbranch_scc1 2f\n\ts_sleep 2\n\t"
                 "s_cmp_lt_u32 %2, 0x100000\n\ts_cbranch_scc1 1b\n2:" : "=&v"(v), "=&s"(sv), "=&s"(n) : "v"(ldsb + SYNC + 8) : "memory", "scc");
  }
  auto sof = [&](int n) { return NH_ST + 2 * (n & 7) + (((n >> 3) ^ (kg >> 1)) & 1); };
  auto wpos = [&](int n) { return n < 12 ? (unsigned)(kg * 24576 + n * 2048) : ring + (unsigned)(n - 12) * 2048; };
#pragma unroll
  for (int n = 0; n < 16; ++n) loadw(wpos(n), sof(n));
  {
    const int hq = lane & 31; const bool early = (hq & 3) == 0 || (hq & 3) == 3;
    gu64* pfo = (gu64*)(ws + WS_FO) + (tm >> 1) * 32 + hq; gu64* done = (gu64*)(ws + WS_FP1) + (tm >> 1) * 32 + hq;
    flags_wait(early ? pfo : done, epoch);
#pragma unroll
    for (int n = 0; n < D; ++n) loada(n % D, sof(n));
#pragma unroll
    for (int n = 0; n < 16; ++n) { const int d = n % D; mma(d, wpos(n), true); SBAR();
      if (n + D == 8) flags_wait(pfo, epoch);
      if (n + D < 16) loada(d, sof(n + D));
      SBAR(); }
  }
  asm volatile("s_barrier" ::: "memory");
  int tz = threadIdx.x; asm volatile("" : "+v"(tz));
  LAS f32x4* red = (LAS f32x4*)lds + (tz & 63);
#pragma unroll
  for (int u = 0; u < 4; ++u)
#pragma unroll
    for (int b = 0; b < 2; ++b)
#pragma unroll
      for (int e = 0; e < 4; ++e) red[((kg * 8 + u * 2 + b) * 4 + e) * 64] = (f32x4){acc[u][b][4 * e], acc[u][b][4 * e + 1], acc[u][b][4 * e + 2], acc[u][b][4 * e + 3]};
  return epoch;
}

__global__ __launch_bounds__(512, 2) void mega(const char* x, const char* wqkv, const char* wo, const char* wg, const char* wu, const char* wd, const char* gatew, char* ws, char* out) {
  extern __shared__ __attribute__((aligned(16))) char smem[];
  LAS char* lds = (LAS char*)smem;
  const int tid = threadIdx.x, lane = tid & 63, wid = __builtin_amdgcn_readfirstlane(tid >> 6);
  const int w = blockIdx.x, xcd = w & 7, i = w >> 3, j = xcd * 4 + (i & 3), r = i >> 2;
  const int tm = (xcd >> 1) * 4 + (i & 3), tn = (xcd & 1) * 8 + (i >> 2);
  if (tid < 4) *(LAS unsigned*)(lds + lm::SYNC + tid * 4) = 0u;
  u64 epoch = 0;
  if (wid < 4) path_attn(lds, x, wqkv, wo, wg, wu, wd, gatew, ws, w, j, r, wid, lane);
  else epoch = path_gemm(lds, x, wqkv, wo, wg, wu, wd, gatew, ws, w, j, r, tm, tn, wid, lane);
  __syncthreads();
  {
    int tz = threadIdx.x; asm volatile("" : "+v"(tz));
    const int ln = tz & 63;
    LAS const f32x4* red = (LAS const f32x4*)lds + ln;
    unsigned pk[4][2];
#pragma unroll
    for (int e = 0; e < 4; ++e) { f32x4 s = red[((0 * 8 + wid) * 4 + e) * 64];
#pragma unroll
      for (int g = 1; g < 4; ++g) s += red[((g * 8 + wid) * 4 + e) * 64];
      pk[e][0] = cvtpk(s[0], s[1]); pk[e][1] = cvtpk(s[2], s[3]); }
    const __amdgpu_buffer_rsrc_t ro = __builtin_amdgcn_make_buffer_rsrc(out, 0, SEQ * DM * 2, 0x00020000);
    const unsigned ob = (unsigned)((tm * 128 + (wid >> 1) * 32 + (ln & 31)) * 2048 + (tn * 64 + (wid & 1) * 32) * 2 + (ln >> 5) * 16);
#pragma unroll
    for (int e = 0; e < 4; e += 2) {
      const auto r0 = __builtin_amdgcn_permlane32_swap(pk[e][0], pk[e + 1][0], false, false), r1 = __builtin_amdgcn_permlane32_swap(pk[e][1], pk[e + 1][1], false, false);
      const u32x4 v = {r0[0], r1[0], r0[1], r1[1]};
      __builtin_amdgcn_raw_buffer_store_b128(v, ro, ob + e * 16, 0, 0);
    }
  }
  if (w == 0 && tid == 256) flag_set(ws, WS_E, epoch);
}

extern "C" void kernel_launch(void* const* d_in, const int* in_sizes, int n_in, void* d_out, int out_size, void* d_ws, size_t ws_size, hipStream_t stream) {
  (void)hipFuncSetAttribute((const void*)mega, hipFuncAttributeMaxDynamicSharedMemorySize, lm::BYTES);
  mega<<<256, 512, lm::BYTES, stream>>>((const char*)d_in[0], (const char*)d_in[1], (const char*)d_in[2], (const char*)d_in[4], (const char*)d_in[5], (const char*)d_in[6],
                                        (const char*)d_in[7], (char*)d_ws, (char*)d_out);
}
```
